```python
import torch

def _softmax(x, axis=-1):
    return torch.softmax(x, dim=axis)
def _gelu(x):
    return torch.nn.functional.gelu(x, approximate="tanh")
def _silu(x):
    return torch.nn.functional.silu(x)
def _sigmoid(x):
    return torch.sigmoid(x)
def _layernorm(x, axis=-1, eps=1e-5):
    m = x.mean(dim=axis, keepdim=True)
    v = x.var(dim=axis, keepdim=True, unbiased=False)
    return (x - m) / torch.sqrt(v + eps)
def _scatter_add(tgt, idx, src, axis=0):
    return tgt.clone().index_add_(axis, idx, src)
def _segment_sum(src, seg, n):
    out = torch.zeros((n,) + src.shape[1:], dtype=src.dtype)
    return out.index_add_(0, seg, src)
def _pad(x, pw):
    flat = tuple(v for pair in reversed(pw) for v in pair)
    return torch.nn.functional.pad(x, flat)

kernel_name = 'standalone__glu_gated_bilinear__decode'

def setup_inputs():

    _w = (lambda a: (lambda t: t.to(torch.bfloat16) if t.is_floating_point() else t)(torch.as_tensor(a)))
    B, S, D, F = 1, 1, 1024, 3584
    return {
        "x": _w((lambda *s: torch.randn(*s, dtype=torch.bfloat16) * 0.05)(B, S, D)),
        "w_gate": _w((lambda *s: torch.randn(*s, dtype=torch.bfloat16) * 0.05)(D, F) * (D ** -0.5)),
        "w_up": _w((lambda *s: torch.randn(*s, dtype=torch.bfloat16) * 0.05)(D, F) * (D ** -0.5)),
        "w_down": _w((lambda *s: torch.randn(*s, dtype=torch.bfloat16) * 0.05)(F, D) * (F ** -0.5)),
    }

def reference(x, w_gate, w_up, w_down):
    g = torch.einsum("bsd,df->bsf", x, w_gate)
    u = torch.einsum("bsd,df->bsf", x, w_up)
    h = g * u
    return torch.einsum("bsf,fd->bsd", h, w_down)
```

```cpp
#include <hip/hip_runtime.h>
#include <hip/hip_bf16.h>

typedef unsigned v4u __attribute__((ext_vector_type(4)));
typedef float v4f __attribute__((ext_vector_type(4)));
#define GAS __attribute__((address_space(1)))

constexpr int D = 1024, F = 3584;
constexpr int S = 16, NWG = F / S;

__device__ __forceinline__ float bflo(unsigned v) { return __builtin_bit_cast(float, v << 16); }
__device__ __forceinline__ float bfhi(unsigned v) { return __builtin_bit_cast(float, v & 0xffff0000u); }
__device__ __forceinline__ float rbf(float f) { return __bfloat162float(__float2bfloat16(f)); }

__global__ void __launch_bounds__(256) k1(const unsigned short* __restrict__ x, const unsigned short* __restrict__ wg,
                                          const unsigned short* __restrict__ wu, const unsigned short* __restrict__ wd,
                                          float* __restrict__ part) {
    __shared__ float red[256 * 16 + 64];
    __shared__ float hs[16];
    const int tid = threadIdx.x;
    const int b = blockIdx.x;
    const int slice = (b & 7) * (NWG / 8) + (b >> 3);
    const int c0 = slice * S;
    const int half = tid & 1, r0 = tid >> 1;
    v4u g[8], u[8], dn[8];
#pragma unroll
    for (int i = 0; i < 8; ++i) g[i] = __builtin_nontemporal_load((const GAS v4u*)(wg + (size_t)(r0 + 128 * i) * F + c0 + 8 * half));
#pragma unroll
    for (int i = 0; i < 8; ++i) u[i] = __builtin_nontemporal_load((const GAS v4u*)(wu + (size_t)(r0 + 128 * i) * F + c0 + 8 * half));
#pragma unroll
    for (int i = 0; i < 8; ++i) dn[i] = __builtin_nontemporal_load((const GAS v4u*)(wd + (size_t)(c0 + (tid >> 7) + 2 * i) * D + 8 * (tid & 127)));
    float xv[8];
#pragma unroll
    for (int i = 0; i < 8; ++i) xv[i] = bflo(x[r0 + 128 * i]);
    __builtin_amdgcn_sched_barrier(0);
    float ag[8] = {0}, au[8] = {0};
#pragma unroll
    for (int i = 0; i < 8; ++i) {
#pragma unroll
        for (int j = 0; j < 4; ++j) {
            ag[2 * j] += xv[i] * bflo(g[i][j]); ag[2 * j + 1] += xv[i] * bfhi(g[i][j]);
        }
    }
#pragma unroll
    for (int i = 0; i < 8; ++i) {
#pragma unroll
        for (int j = 0; j < 4; ++j) {
            au[2 * j] += xv[i] * bflo(u[i][j]); au[2 * j + 1] += xv[i] * bfhi(u[i][j]);
        }
    }
#pragma unroll
    for (int j = 0; j < 8; ++j) {
#pragma unroll
        for (int m = 2; m < 64; m <<= 1) { ag[j] += __shfl_xor(ag[j], m); au[j] += __shfl_xor(au[j], m); }
    }
    const int lane = tid & 63, w = tid >> 6;
    if (lane < 2) {
#pragma unroll
        for (int j = 0; j < 8; ++j) { red[w * 32 + lane * 8 + j] = ag[j]; red[w * 32 + 16 + lane * 8 + j] = au[j]; }
    }
    __syncthreads();
    if (tid < 16) {
        float gs = red[tid] + red[32 + tid] + red[64 + tid] + red[96 + tid];
        float us = red[16 + tid] + red[48 + tid] + red[80 + tid] + red[112 + tid];
        hs[tid] = rbf(rbf(gs) * rbf(us));
    }
    __syncthreads();
    float o[8] = {0};
#pragma unroll
    for (int i = 0; i < 8; ++i) {
        const float hv = hs[(tid >> 7) + 2 * i];
#pragma unroll
        for (int j = 0; j < 4; ++j) { o[2 * j] += hv * bflo(dn[i][j]); o[2 * j + 1] += hv * bfhi(dn[i][j]); }
    }
    __syncthreads();
    if (tid >= 128) {
#pragma unroll
        for (int j = 0; j < 8; ++j) red[(tid - 128) * 8 + j] = o[j];
    }
    __syncthreads();
    if (tid < 128) {
        v4f a, bb;
#pragma unroll
        for (int j = 0; j < 4; ++j) { a[j] = o[j] + red[tid * 8 + j]; bb[j] = o[4 + j] + red[tid * 8 + 4 + j]; }
        *(v4f*)(part + (size_t)slice * D + tid * 8) = a;
        *(v4f*)(part + (size_t)slice * D + tid * 8 + 4) = bb;
    }
}

__global__ void __launch_bounds__(256) k2(const float* __restrict__ part, __hip_bfloat16* __restrict__ out) {
    __shared__ float red[4 * 16];
    const int tid = threadIdx.x, c0 = blockIdx.x * 16 + (tid & 3) * 4, r0 = tid >> 2;
    v4f acc = {0, 0, 0, 0};
    v4f v[4];
#pragma unroll
    for (int i = 0; i < 4; ++i) { const int r = r0 + 64 * i; v[i] = r < NWG ? *(const v4f*)(part + (size_t)r * D + c0) : (v4f){0, 0, 0, 0}; }
#pragma unroll
    for (int i = 0; i < 4; ++i) acc += v[i];
#pragma unroll
    for (int j = 0; j < 4; ++j)
#pragma unroll
        for (int m = 4; m < 64; m <<= 1) acc[j] += __shfl_xor(acc[j], m);
    const int lane = tid & 63, w = tid >> 6;
    if (lane < 4) {
#pragma unroll
        for (int j = 0; j < 4; ++j) red[w * 16 + lane * 4 + j] = acc[j];
    }
    __syncthreads();
    if (tid < 16) out[blockIdx.x * 16 + tid] = __float2bfloat16(red[tid] + red[16 + tid] + red[32 + tid] + red[48 + tid]);
}

extern "C" void kernel_launch(void* const* d_in, const int* in_sizes, int n_in, void* d_out, int out_size, void* d_ws, size_t ws_size,
                              hipStream_t stream) {
    const unsigned short* x = (const unsigned short*)d_in[0];
    const unsigned short* wg = (const unsigned short*)d_in[1];
    const unsigned short* wu = (const unsigned short*)d_in[2];
    const unsigned short* wd = (const unsigned short*)d_in[3];
    k1<<<NWG, 256, 0, stream>>>(x, wg, wu, wd, (float*)d_ws);
    k2<<<64, 256, 0, stream>>>((const float*)d_ws, (__hip_bfloat16*)d_out);
}
```

```cpp
#include <hip/hip_runtime.h>
#include <hip/hip_bf16.h>

typedef unsigned v4u __attribute__((ext_vector_type(4)));
typedef unsigned v2u __attribute__((ext_vector_type(2)));
typedef float v4f __attribute__((ext_vector_type(4)));
typedef float v2f __attribute__((ext_vector_type(2)));
typedef short bf16x8 __attribute__((ext_vector_type(8)));
typedef __bf16 bf2 __attribute__((ext_vector_type(2)));
#define GAS __attribute__((address_space(1)))
#define RLX_AGENT __ATOMIC_RELAXED, __HIP_MEMORY_SCOPE_AGENT

#define DOWN_AT 16
#define STAMPS 0

constexpr int D = 1024, F = 3584;
constexpr int NS = 56, NWG = 224;
constexpr int T1 = 16;
constexpr int T2 = 8;
constexpr unsigned EP_OFF = 0;
constexpr unsigned G1_OFF = 4096;
constexpr unsigned G2_OFF = G1_OFF + NWG * 1024;
constexpr unsigned WS_BYTES = G2_OFF + 128 * NS * 64;
constexpr unsigned ST_OFF = 1u << 20;
constexpr unsigned SPIN_CAP = 400000;

__device__ __forceinline__ v4u ldnt(const void* p) { return __builtin_nontemporal_load((const GAS v4u*)p); }
__device__ __forceinline__ v4f mfma(v4u a, v4u b, v4f c) {
    return __builtin_amdgcn_mfma_f32_16x16x32_bf16(__builtin_bit_cast(bf16x8, a), __builtin_bit_cast(bf16x8, b), c, 0, 0, 0);
}
__device__ __forceinline__ float f_of(unsigned u) { return __builtin_bit_cast(float, u); }
__device__ __forceinline__ unsigned u_of(float f) { return __builtin_bit_cast(unsigned, f); }
__device__ __forceinline__ unsigned pack_bf2(float lo, float hi) { bf2 r; r.x = (__bf16)lo; r.y = (__bf16)hi; return __builtin_bit_cast(unsigned, r); }
template <int CTRL> __device__ __forceinline__ float dpp(float v) {
    return f_of((unsigned)__builtin_amdgcn_update_dpp(0, (int)u_of(v), CTRL, 0xf, 0xf, false));
}
#define STAMP(i) do {} while (0)

__global__ void __launch_bounds__(320) glu_kernel(const unsigned short* __restrict__ x, const unsigned short* __restrict__ wg,
                                                  const unsigned short* __restrict__ wu, const unsigned short* __restrict__ wd,
                                                  unsigned* __restrict__ out, unsigned char* ws) {
    __shared__ __attribute__((aligned(16))) float lds[4 * 128 + 32 + 4];
    STAMP(0);
    const int lane = threadIdx.x & 63;
    const int w = __builtin_amdgcn_readfirstlane(threadIdx.x >> 6);
    const int b = blockIdx.x;
    const int id = (b & 7) * (NWG / 8) + (b >> 3);
    const int s = id >> 2, q = id & 3;
    const __amdgpu_buffer_rsrc_t rs = __builtin_amdgcn_make_buffer_rsrc((void*)ws, 0, WS_BYTES, 0x00020000);

    if (w < 4) {
        const int g = lane >> 4, col = lane & 15;
        const int row0 = q * 256 + w * 64 + g * T1;
        const unsigned short* src = (col < 8 ? wg : wu) + (size_t)row0 * F + s * 64 + (col & 7) * 8;
        const v4u xa = *(const GAS v4u*)(x + row0), xb = *(const GAS v4u*)(x + row0 + 8);
        v4u a[T1];
#pragma unroll
        for (int t = 0; t < T1; ++t) a[t] = ldnt(src + (size_t)t * F);
        const unsigned short* dsrc = wd + (size_t)(s * 64 + 8 * (lane >> 3)) * D + q * 256 + w * 64 + (lane & 7) * 8;
        v4u dn[T2];
        if (DOWN_AT == 0) {
#pragma unroll
            for (int t = 0; t < T2; ++t) dn[t] = ldnt(dsrc + (size_t)t * D);
        }
        __builtin_amdgcn_sched_barrier(0);
        STAMP(1);
        const int jp = lane & 7;
        unsigned sel[4][2];
#pragma unroll
        for (int r = 0; r < 4; ++r)
#pragma unroll
            for (int p = 0; p < 2; ++p) {
                const unsigned hsel = p ? 0x0302u : 0x0100u;
                const unsigned on = (jp & 1) ? ((hsel << 16) | 0x0c0cu) : (0x0c0c0000u | hsel);
                sel[r][p] = ((jp >> 1) == r) ? on : 0x0c0c0c0cu;
            }
        const unsigned xw[8] = {xa.x, xa.y, xa.z, xa.w, xb.x, xb.y, xb.z, xb.w};
        v4f acc = {0.f, 0.f, 0.f, 0.f};
#pragma unroll
        for (int t = 0; t < T1; ++t) {
            v4u av;
#pragma unroll
            for (int r = 0; r < 4; ++r) av[r] = __builtin_amdgcn_perm(0u, xw[t >> 1], sel[r][t & 1]);
            acc = mfma(av, a[t], acc);
            if (t + 1 == DOWN_AT) {
                __builtin_amdgcn_sched_barrier(0);
#pragma unroll
                for (int tt = 0; tt < T2; ++tt) dn[tt] = ldnt(dsrc + (size_t)tt * D);
                __builtin_amdgcn_sched_barrier(0);
            }
        }
        if (lane < 32) *(v4f*)&lds[w * 128 + col * 8 + 4 * g] = acc;
        STAMP(2);
        __syncthreads();
        __syncthreads();
        STAMP(3);
        const v4u hv = *(const v4u*)&lds[512 + 4 * (lane >> 3)];
        const v2u tg = *(const v2u*)&lds[512 + 32];
        v4f o = {0.f, 0.f, 0.f, 0.f};
#pragma unroll
        for (int t = 0; t < T2; ++t) {
            v4u av;
#pragma unroll
            for (int r = 0; r < 4; ++r) av[r] = __builtin_amdgcn_perm(0u, hv[t >> 1], sel[r][t & 1]);
            o = mfma(av, dn[t], o);
        }
        auto s02 = __builtin_amdgcn_permlane32_swap(u_of(o[0]), u_of(o[2]), false, false);
        auto s13 = __builtin_amdgcn_permlane32_swap(u_of(o[1]), u_of(o[3]), false, false);
        const float v0 = f_of(s02[0]) + dpp<0x128>(f_of(s02[1]));
        const float v1 = f_of(s13[0]) + dpp<0x128>(f_of(s13[1]));
        if (col < 8) {
            const v4u gr = {u_of(v0), tg.x, u_of(v1), tg.y};
            const unsigned off = G2_OFF + (unsigned)(((q * 4 + w) * 8 + col) * NS + s) * 64u + (unsigned)g * 16u;
            __builtin_amdgcn_raw_buffer_store_b128(gr, rs, off, 0, 16);
        }
        STAMP(4);
    } else {
        const unsigned long long ep = __hip_atomic_load((GAS unsigned long long*)(ws + EP_OFF), RLX_AGENT);
        const unsigned tagA = (unsigned)ep + 1u, tagB = (unsigned)(ep >> 32) + 1u;
        __syncthreads();
        STAMP(1);
        v2f p = *(const v2f*)&lds[2 * lane];
        {
            const v2f p1 = *(const v2f*)&lds[128 + 2 * lane], p2 = *(const v2f*)&lds[256 + 2 * lane], p3 = *(const v2f*)&lds[384 + 2 * lane];
            p = (p + p1) + (p2 + p3);
        }
        {
            const v4u gr = {u_of(p.x), tagA, u_of(p.y), tagB};
            __builtin_amdgcn_raw_buffer_store_b128(gr, rs, G1_OFF + (unsigned)id * 1024u + (unsigned)lane * 16u, 0, 16);
        }
        STAMP(2);
        v4u o1, o2, o3;
        for (unsigned spins = 0;; ++spins) {
            asm volatile("" ::: "memory");
            o1 = __builtin_amdgcn_raw_buffer_load_b128(rs, G1_OFF + (unsigned)(id ^ 1) * 1024u + (unsigned)lane * 16u, 0, 16);
            o2 = __builtin_amdgcn_raw_buffer_load_b128(rs, G1_OFF + (unsigned)(id ^ 2) * 1024u + (unsigned)lane * 16u, 0, 16);
            o3 = __builtin_amdgcn_raw_buffer_load_b128(rs, G1_OFF + (unsigned)(id ^ 3) * 1024u + (unsigned)lane * 16u, 0, 16);
            const bool ok = o1.y == tagA && o1.w == tagB && o2.y == tagA && o2.w == tagB && o3.y == tagA && o3.w == tagB;
            if (__all(ok) || spins > SPIN_CAP) break;
        }
        STAMP(3);
        const float s0 = (p.x + f_of(o1.x)) + (f_of(o2.x) + f_of(o3.x));
        const float s1 = (p.y + f_of(o1.z)) + (f_of(o2.z) + f_of(o3.z));
        const unsigned mine = pack_bf2(s0, s1);
        auto sw = __builtin_amdgcn_permlane32_swap(mine, mine, false, false);
        const unsigned gq = sw[0], uq = sw[1];
        const float h0 = f_of(gq << 16) * f_of(uq << 16), h1 = f_of(gq & 0xffff0000u) * f_of(uq & 0xffff0000u);
        if (lane < 32) ((unsigned*)lds)[512 + lane] = pack_bf2(h0, h1);
        else if (lane < 34) ((unsigned*)lds)[512 + lane] = lane == 32 ? tagA : tagB;
        __syncthreads();
        STAMP(4);
        if (b < 128) {
            const unsigned base = G2_OFF + (unsigned)b * (NS * 64u) + (unsigned)lane * 16u;
            v4u r0, r1, r2, r3;
            for (unsigned spins = 0;; ++spins) {
                asm volatile("" ::: "memory");
                r0 = __builtin_amdgcn_raw_buffer_load_b128(rs, base, 0, 16);
                r1 = __builtin_amdgcn_raw_buffer_load_b128(rs, base + 1024u, 0, 16);
                r2 = __builtin_amdgcn_raw_buffer_load_b128(rs, base + 2048u, 0, 16);
                r3 = __builtin_amdgcn_raw_buffer_load_b128(rs, base + (lane < 32 ? 3072u : 0u), 0, 16);
                const bool ok = r0.y == tagA && r0.w == tagB && r1.y == tagA && r1.w == tagB && r2.y == tagA && r2.w == tagB && r3.y == tagA && r3.w == tagB;
                if (__all(ok) || spins > SPIN_CAP) break;
            }
            STAMP(5);
            float a0 = (f_of(r0.x) + f_of(r1.x)) + (f_of(r2.x) + (lane < 32 ? f_of(r3.x) : 0.f));
            float a1 = (f_of(r0.z) + f_of(r1.z)) + (f_of(r2.z) + (lane < 32 ? f_of(r3.z) : 0.f));
            a0 += dpp<0x124>(a0); a1 += dpp<0x124>(a1);
            a0 += dpp<0x128>(a0); a1 += dpp<0x128>(a1);
            auto sx = __builtin_amdgcn_permlane32_swap(u_of(a0), u_of(a1), false, false);
            float c = f_of(sx[0]) + f_of(sx[1]);
            auto sy = __builtin_amdgcn_permlane16_swap(u_of(c), u_of(c), false, false);
            c = f_of(sy[0]) + f_of(sy[1]);
            auto sz = __builtin_amdgcn_permlane32_swap(u_of(c), u_of(c), false, false);
            if (lane < 4) {
                const int pos = ((lane & 1) << 1) | (lane >> 1);
                out[b * 4 + pos] = pack_bf2(f_of(sz[0]), f_of(sz[1]));
            }
            if (b == 0 && lane == 0) __hip_atomic_store((GAS unsigned long long*)(ws + EP_OFF), ((unsigned long long)tagB << 32) | tagA, RLX_AGENT);
            STAMP(6);
        }
    }
}

extern "C" void kernel_launch(void* const* d_in, const int* in_sizes, int n_in, void* d_out, int out_size, void* d_ws, size_t ws_size,
                              hipStream_t stream) {
    glu_kernel<<<NWG, 320, 0, stream>>>((const unsigned short*)d_in[0], (const unsigned short*)d_in[1], (const unsigned short*)d_in[2],
                                        (const unsigned short*)d_in[3], (unsigned*)d_out, (unsigned char*)d_ws);
}
```

```cpp
#include <hip/hip_runtime.h>
#include <hip/hip_bf16.h>

typedef unsigned v4u __attribute__((ext_vector_type(4)));
typedef unsigned v2u __attribute__((ext_vector_type(2)));
typedef float v4f __attribute__((ext_vector_type(4)));
typedef float v2f __attribute__((ext_vector_type(2)));
typedef short bf16x8 __attribute__((ext_vector_type(8)));
typedef __bf16 bf2 __attribute__((ext_vector_type(2)));
#define GAS __attribute__((address_space(1)))
#define RLX_AGENT __ATOMIC_RELAXED, __HIP_MEMORY_SCOPE_AGENT

#define DOWN_AT 8
#define STAMPS 1

constexpr int D = 1024, F = 3584;
constexpr int NS = 56, NWG = 224;
constexpr int T1 = 16;
constexpr int T2 = 8;
constexpr unsigned EP_OFF = 0;
constexpr unsigned G1_OFF = 4096;
constexpr unsigned G2_OFF = G1_OFF + NWG * 1024;
constexpr unsigned WS_BYTES = G2_OFF + 128 * NS * 64;
constexpr unsigned ST_OFF = 1u << 20;
constexpr unsigned SPIN_CAP = 400000;

__device__ __forceinline__ v4u ldnt(const void* p) { return __builtin_nontemporal_load((const GAS v4u*)p); }
__device__ __forceinline__ v4f mfma(v4u a, v4u b, v4f c) {
    return __builtin_amdgcn_mfma_f32_16x16x32_bf16(__builtin_bit_cast(bf16x8, a), __builtin_bit_cast(bf16x8, b), c, 0, 0, 0);
}
__device__ __forceinline__ float f_of(unsigned u) { return __builtin_bit_cast(float, u); }
__device__ __forceinline__ unsigned u_of(float f) { return __builtin_bit_cast(unsigned, f); }
__device__ __forceinline__ unsigned pack_bf2(float lo, float hi) { bf2 r; r.x = (__bf16)lo; r.y = (__bf16)hi; return __builtin_bit_cast(unsigned, r); }
template <int CTRL> __device__ __forceinline__ float dpp(float v) {
    return f_of((unsigned)__builtin_amdgcn_update_dpp(0, (int)u_of(v), CTRL, 0xf, 0xf, false));
}
#define STAMP(i) do { __builtin_amdgcn_sched_barrier(0); st[i] = __builtin_amdgcn_s_memrealtime(); __builtin_amdgcn_sched_barrier(0); } while (0)

__global__ void __launch_bounds__(320) glu_kernel(const unsigned short* __restrict__ x, const unsigned short* __restrict__ wg,
                                                  const unsigned short* __restrict__ wu, const unsigned short* __restrict__ wd,
                                                  unsigned* __restrict__ out, unsigned char* ws) {
    __shared__ __attribute__((aligned(16))) float lds[4 * 128 + 32 + 4];
    unsigned long long st[8] = {0, 0, 0, 0, 0, 0, 0, 0};
    STAMP(0);
    const int lane = threadIdx.x & 63;
    const int w = __builtin_amdgcn_readfirstlane(threadIdx.x >> 6);
    const int b = blockIdx.x;
    const int id = (b & 7) * (NWG / 8) + (b >> 3);
    const int s = id >> 2, q = id & 3;
    const __amdgpu_buffer_rsrc_t rs = __builtin_amdgcn_make_buffer_rsrc((void*)ws, 0, WS_BYTES, 0x00020000);

    if (w < 4) {
        const int g = lane >> 4, col = lane & 15;
        const int row0 = q * 256 + w * 64 + g * T1;
        const unsigned short* src = (col < 8 ? wg : wu) + (size_t)row0 * F + s * 64 + (col & 7) * 8;
        const v4u xa = *(const GAS v4u*)(x + row0), xb = *(const GAS v4u*)(x + row0 + 8);
        v4u a[T1];
#pragma unroll
        for (int t = 0; t < T1; ++t) a[t] = ldnt(src + (size_t)t * F);
        const unsigned short* dsrc = wd + (size_t)(s * 64 + 8 * (lane >> 3)) * D + q * 256 + w * 64 + (lane & 7) * 8;
        v4u dn[T2];
        if (DOWN_AT == 0) {
#pragma unroll
            for (int t = 0; t < T2; ++t) dn[t] = ldnt(dsrc + (size_t)t * D);
        }
        __builtin_amdgcn_sched_barrier(0);
        STAMP(1);
        const int jp = lane & 7;
        unsigned sel[4][2];
#pragma unroll
        for (int r = 0; r < 4; ++r)
#pragma unroll
            for (int p = 0; p < 2; ++p) {
                const unsigned hsel = p ? 0x0302u : 0x0100u;
                const unsigned on = (jp & 1) ? ((hsel << 16) | 0x0c0cu) : (0x0c0c0000u | hsel);
                sel[r][p] = ((jp >> 1) == r) ? on : 0x0c0c0c0cu;
            }
        const unsigned xw[8] = {xa.x, xa.y, xa.z, xa.w, xb.x, xb.y, xb.z, xb.w};
        v4f acc = {0.f, 0.f, 0.f, 0.f};
#pragma unroll
        for (int t = 0; t < T1; ++t) {
            v4u av;
#pragma unroll
            for (int r = 0; r < 4; ++r) av[r] = __builtin_amdgcn_perm(0u, xw[t >> 1], sel[r][t & 1]);
            acc = mfma(av, a[t], acc);
            if (t + 1 == DOWN_AT) {
                __builtin_amdgcn_sched_barrier(0);
#pragma unroll
                for (int tt = 0; tt < T2; ++tt) dn[tt] = ldnt(dsrc + (size_t)tt * D);
                __builtin_amdgcn_sched_barrier(0);
            }
        }
        if (lane < 32) *(v4f*)&lds[w * 128 + col * 8 + 4 * g] = acc;
        STAMP(2);
        __syncthreads();
        __syncthreads();
        STAMP(3);
        const v4u hv = *(const v4u*)&lds[512 + 4 * (lane >> 3)];
        const v2u tg = *(const v2u*)&lds[512 + 32];
        v4f o = {0.f, 0.f, 0.f, 0.f};
#pragma unroll
        for (int t = 0; t < T2; ++t) {
            v4u av;
#pragma unroll
            for (int r = 0; r < 4; ++r) av[r] = __builtin_amdgcn_perm(0u, hv[t >> 1], sel[r][t & 1]);
            o = mfma(av, dn[t], o);
        }
        auto s02 = __builtin_amdgcn_permlane32_swap(u_of(o[0]), u_of(o[2]), false, false);
        auto s13 = __builtin_amdgcn_permlane32_swap(u_of(o[1]), u_of(o[3]), false, false);
        const float v0 = f_of(s02[0]) + dpp<0x128>(f_of(s02[1]));
        const float v1 = f_of(s13[0]) + dpp<0x128>(f_of(s13[1]));
        if (col < 8) {
            const v4u gr = {u_of(v0), tg.x, u_of(v1), tg.y};
            const unsigned off = G2_OFF + (unsigned)(((q * 4 + w) * 8 + col) * NS + s) * 64u + (unsigned)g * 16u;
            __builtin_amdgcn_raw_buffer_store_b128(gr, rs, off, 0, 16);
        }
        STAMP(4);
    } else {
        const unsigned long long ep = __hip_atomic_load((GAS unsigned long long*)(ws + EP_OFF), RLX_AGENT);
        const unsigned tagA = (unsigned)ep + 1u, tagB = (unsigned)(ep >> 32) + 1u;
        __syncthreads();
        STAMP(1);
        v2f p = *(const v2f*)&lds[2 * lane];
        {
            const v2f p1 = *(const v2f*)&lds[128 + 2 * lane], p2 = *(const v2f*)&lds[256 + 2 * lane], p3 = *(const v2f*)&lds[384 + 2 * lane];
            p = (p + p1) + (p2 + p3);
        }
        {
            const v4u gr = {u_of(p.x), tagA, u_of(p.y), tagB};
            __builtin_amdgcn_raw_buffer_store_b128(gr, rs, G1_OFF + (unsigned)id * 1024u + (unsigned)lane * 16u, 0, 16);
        }
        STAMP(2);
        v4u o1, o2, o3;
        for (unsigned spins = 0;; ++spins) {
            asm volatile("" ::: "memory");
            o1 = __builtin_amdgcn_raw_buffer_load_b128(rs, G1_OFF + (unsigned)(id ^ 1) * 1024u + (unsigned)lane * 16u, 0, 16);
            o2 = __builtin_amdgcn_raw_buffer_load_b128(rs, G1_OFF + (unsigned)(id ^ 2) * 1024u + (unsigned)lane * 16u, 0, 16);
            o3 = __builtin_amdgcn_raw_buffer_load_b128(rs, G1_OFF + (unsigned)(id ^ 3) * 1024u + (unsigned)lane * 16u, 0, 16);
            const bool ok = o1.y == tagA && o1.w == tagB && o2.y == tagA && o2.w == tagB && o3.y == tagA && o3.w == tagB;
            if (__all(ok) || spins > SPIN_CAP) break;
        }
        STAMP(3);
        const float s0 = (p.x + f_of(o1.x)) + (f_of(o2.x) + f_of(o3.x));
        const float s1 = (p.y + f_of(o1.z)) + (f_of(o2.z) + f_of(o3.z));
        const unsigned mine = pack_bf2(s0, s1);
        auto sw = __builtin_amdgcn_permlane32_swap(mine, mine, false, false);
        const unsigned gq = sw[0], uq = sw[1];
        const float h0 = f_of(gq << 16) * f_of(uq << 16), h1 = f_of(gq & 0xffff0000u) * f_of(uq & 0xffff0000u);
        if (lane < 32) ((unsigned*)lds)[512 + lane] = pack_bf2(h0, h1);
        else if (lane < 34) ((unsigned*)lds)[512 + lane] = lane == 32 ? tagA : tagB;
        __syncthreads();
        STAMP(4);
        if (b < 128) {
            const unsigned base = G2_OFF + (unsigned)b * (NS * 64u) + (unsigned)lane * 16u;
            v4u r0, r1, r2, r3;
            for (unsigned spins = 0;; ++spins) {
                asm volatile("" ::: "memory");
                r0 = __builtin_amdgcn_raw_buffer_load_b128(rs, base, 0, 16);
                r1 = __builtin_amdgcn_raw_buffer_load_b128(rs, base + 1024u, 0, 16);
                r2 = __builtin_amdgcn_raw_buffer_load_b128(rs, base + 2048u, 0, 16);
                r3 = __builtin_amdgcn_raw_buffer_load_b128(rs, base + (lane < 32 ? 3072u : 0u), 0, 16);
                const bool ok = r0.y == tagA && r0.w == tagB && r1.y == tagA && r1.w == tagB && r2.y == tagA && r2.w == tagB && r3.y == tagA && r3.w == tagB;
                if (__all(ok) || spins > SPIN_CAP) break;
            }
            STAMP(5);
            float a0 = (f_of(r0.x) + f_of(r1.x)) + (f_of(r2.x) + (lane < 32 ? f_of(r3.x) : 0.f));
            float a1 = (f_of(r0.z) + f_of(r1.z)) + (f_of(r2.z) + (lane < 32 ? f_of(r3.z) : 0.f));
            a0 += dpp<0x124>(a0); a1 += dpp<0x124>(a1);
            a0 += dpp<0x128>(a0); a1 += dpp<0x128>(a1);
            auto sx = __builtin_amdgcn_permlane32_swap(u_of(a0), u_of(a1), false, false);
            float c = f_of(sx[0]) + f_of(sx[1]);
            auto sy = __builtin_amdgcn_permlane16_swap(u_of(c), u_of(c), false, false);
            c = f_of(sy[0]) + f_of(sy[1]);
            auto sz = __builtin_amdgcn_permlane32_swap(u_of(c), u_of(c), false, false);
            if (lane < 4) {
                const int pos = ((lane & 1) << 1) | (lane >> 1);
                out[b * 4 + pos] = pack_bf2(f_of(sz[0]), f_of(sz[1]));
            }
            if (b == 0 && lane == 0) __hip_atomic_store((GAS unsigned long long*)(ws + EP_OFF), ((unsigned long long)tagB << 32) | tagA, RLX_AGENT);
            STAMP(6);
        }
    }
    if (b == 0 && w == 4) {
        const unsigned long long iv = st[0] - st[0];
        const unsigned long long until = __builtin_amdgcn_s_memrealtime() + 10ull * iv;
        while (__builtin_amdgcn_s_memrealtime() < until) {}
    }

}

extern "C" void kernel_launch(void* const* d_in, const int* in_sizes, int n_in, void* d_out, int out_size, void* d_ws, size_t ws_size,
                              hipStream_t stream) {
    glu_kernel<<<NWG, 320, 0, stream>>>((const unsigned short*)d_in[0], (const unsigned short*)d_in[1], (const unsigned short*)d_in[2],
                                        (const unsigned short*)d_in[3], (unsigned*)d_out, (unsigned char*)d_ws);
}
```

```cpp
#include <hip/hip_runtime.h>
#include <hip/hip_bf16.h>

typedef unsigned v4u __attribute__((ext_vector_type(4)));
typedef unsigned v2u __attribute__((ext_vector_type(2)));
typedef float v4f __attribute__((ext_vector_type(4)));
typedef float v2f __attribute__((ext_vector_type(2)));
typedef short bf16x8 __attribute__((ext_vector_type(8)));
typedef __bf16 bf2 __attribute__((ext_vector_type(2)));
#define GAS __attribute__((address_space(1)))
#define RLX_AGENT __ATOMIC_RELAXED, __HIP_MEMORY_SCOPE_AGENT

#define DOWN_AT 8
#define STAMPS 1

constexpr int D = 1024, F = 3584;
constexpr int NS = 56, NWG = 224;
constexpr int T1 = 16;
constexpr int T2 = 8;
constexpr unsigned EP_OFF = 0;
constexpr unsigned G1_OFF = 4096;
constexpr unsigned G2_OFF = G1_OFF + NWG * 1024;
constexpr unsigned WS_BYTES = G2_OFF + 128 * NS * 64;
constexpr unsigned ST_OFF = 1u << 20;
constexpr unsigned SPIN_CAP = 400000;

__device__ __forceinline__ v4u ldnt(const void* p) { return __builtin_nontemporal_load((const GAS v4u*)p); }
__device__ __forceinline__ v4f mfma(v4u a, v4u b, v4f c) {
    return __builtin_amdgcn_mfma_f32_16x16x32_bf16(__builtin_bit_cast(bf16x8, a), __builtin_bit_cast(bf16x8, b), c, 0, 0, 0);
}
__device__ __forceinline__ float f_of(unsigned u) { return __builtin_bit_cast(float, u); }
__device__ __forceinline__ unsigned u_of(float f) { return __builtin_bit_cast(unsigned, f); }
__device__ __forceinline__ unsigned pack_bf2(float lo, float hi) { bf2 r; r.x = (__bf16)lo; r.y = (__bf16)hi; return __builtin_bit_cast(unsigned, r); }
template <int CTRL> __device__ __forceinline__ float dpp(float v) {
    return f_of((unsigned)__builtin_amdgcn_update_dpp(0, (int)u_of(v), CTRL, 0xf, 0xf, false));
}
#define STAMP(i) do { __builtin_amdgcn_sched_barrier(0); st[i] = __builtin_amdgcn_s_memrealtime(); __builtin_amdgcn_sched_barrier(0); } while (0)

__global__ void __launch_bounds__(320) glu_kernel(const unsigned short* __restrict__ x, const unsigned short* __restrict__ wg,
                                                  const unsigned short* __restrict__ wu, const unsigned short* __restrict__ wd,
                                                  unsigned* __restrict__ out, unsigned char* ws) {
    __shared__ __attribute__((aligned(16))) float lds[4 * 128 + 32 + 4];
    unsigned long long st[8] = {0, 0, 0, 0, 0, 0, 0, 0};
    STAMP(0);
    const int lane = threadIdx.x & 63;
    const int w = __builtin_amdgcn_readfirstlane(threadIdx.x >> 6);
    const int b = blockIdx.x;
    const int id = (b & 7) * (NWG / 8) + (b >> 3);
    const int s = id >> 2, q = id & 3;
    const __amdgpu_buffer_rsrc_t rs = __builtin_amdgcn_make_buffer_rsrc((void*)ws, 0, WS_BYTES, 0x00020000);

    if (w < 4) {
        const int g = lane >> 4, col = lane & 15;
        const int row0 = q * 256 + w * 64 + g * T1;
        const unsigned short* src = (col < 8 ? wg : wu) + (size_t)row0 * F + s * 64 + (col & 7) * 8;
        const v4u xa = *(const GAS v4u*)(x + row0), xb = *(const GAS v4u*)(x + row0 + 8);
        v4u a[T1];
#pragma unroll
        for (int t = 0; t < T1; ++t) a[t] = ldnt(src + (size_t)t * F);
        const unsigned short* dsrc = wd + (size_t)(s * 64 + 8 * (lane >> 3)) * D + q * 256 + w * 64 + (lane & 7) * 8;
        v4u dn[T2];
        if (DOWN_AT == 0) {
#pragma unroll
            for (int t = 0; t < T2; ++t) dn[t] = ldnt(dsrc + (size_t)t * D);
        }
        __builtin_amdgcn_sched_barrier(0);
        STAMP(1);
        const int jp = lane & 7;
        unsigned sel[4][2];
#pragma unroll
        for (int r = 0; r < 4; ++r)
#pragma unroll
            for (int p = 0; p < 2; ++p) {
                const unsigned hsel = p ? 0x0302u : 0x0100u;
                const unsigned on = (jp & 1) ? ((hsel << 16) | 0x0c0cu) : (0x0c0c0000u | hsel);
                sel[r][p] = ((jp >> 1) == r) ? on : 0x0c0c0c0cu;
            }
        const unsigned xw[8] = {xa.x, xa.y, xa.z, xa.w, xb.x, xb.y, xb.z, xb.w};
        v4f acc = {0.f, 0.f, 0.f, 0.f};
#pragma unroll
        for (int t = 0; t < T1; ++t) {
            v4u av;
#pragma unroll
            for (int r = 0; r < 4; ++r) av[r] = __builtin_amdgcn_perm(0u, xw[t >> 1], sel[r][t & 1]);
            acc = mfma(av, a[t], acc);
            if (t + 1 == DOWN_AT) {
                __builtin_amdgcn_sched_barrier(0);
#pragma unroll
                for (int tt = 0; tt < T2; ++tt) dn[tt] = ldnt(dsrc + (size_t)tt * D);
                __builtin_amdgcn_sched_barrier(0);
            }
        }
        if (lane < 32) *(v4f*)&lds[w * 128 + col * 8 + 4 * g] = acc;
        STAMP(2);
        __syncthreads();
        __syncthreads();
        STAMP(3);
        const v4u hv = *(const v4u*)&lds[512 + 4 * (lane >> 3)];
        const v2u tg = *(const v2u*)&lds[512 + 32];
        v4f o = {0.f, 0.f, 0.f, 0.f};
#pragma unroll
        for (int t = 0; t < T2; ++t) {
            v4u av;
#pragma unroll
            for (int r = 0; r < 4; ++r) av[r] = __builtin_amdgcn_perm(0u, hv[t >> 1], sel[r][t & 1]);
            o = mfma(av, dn[t], o);
        }
        auto s02 = __builtin_amdgcn_permlane32_swap(u_of(o[0]), u_of(o[2]), false, false);
        auto s13 = __builtin_amdgcn_permlane32_swap(u_of(o[1]), u_of(o[3]), false, false);
        const float v0 = f_of(s02[0]) + dpp<0x128>(f_of(s02[1]));
        const float v1 = f_of(s13[0]) + dpp<0x128>(f_of(s13[1]));
        if (col < 8) {
            const v4u gr = {u_of(v0), tg.x, u_of(v1), tg.y};
            const unsigned off = G2_OFF + (unsigned)(((q * 4 + w) * 8 + col) * NS + s) * 64u + (unsigned)g * 16u;
            __builtin_amdgcn_raw_buffer_store_b128(gr, rs, off, 0, 16);
        }
        STAMP(4);
    } else {
        const unsigned long long ep = __hip_atomic_load((GAS unsigned long long*)(ws + EP_OFF), RLX_AGENT);
        const unsigned tagA = (unsigned)ep + 1u, tagB = (unsigned)(ep >> 32) + 1u;
        __syncthreads();
        STAMP(1);
        v2f p = *(const v2f*)&lds[2 * lane];
        {
            const v2f p1 = *(const v2f*)&lds[128 + 2 * lane], p2 = *(const v2f*)&lds[256 + 2 * lane], p3 = *(const v2f*)&lds[384 + 2 * lane];
            p = (p + p1) + (p2 + p3);
        }
        {
            const v4u gr = {u_of(p.x), tagA, u_of(p.y), tagB};
            __builtin_amdgcn_raw_buffer_store_b128(gr, rs, G1_OFF + (unsigned)id * 1024u + (unsigned)lane * 16u, 0, 16);
        }
        STAMP(2);
        v4u o1, o2, o3;
        for (unsigned spins = 0;; ++spins) {
            asm volatile("" ::: "memory");
            o1 = __builtin_amdgcn_raw_buffer_load_b128(rs, G1_OFF + (unsigned)(id ^ 1) * 1024u + (unsigned)lane * 16u, 0, 16);
            o2 = __builtin_amdgcn_raw_buffer_load_b128(rs, G1_OFF + (unsigned)(id ^ 2) * 1024u + (unsigned)lane * 16u, 0, 16);
            o3 = __builtin_amdgcn_raw_buffer_load_b128(rs, G1_OFF + (unsigned)(id ^ 3) * 1024u + (unsigned)lane * 16u, 0, 16);
            const bool ok = o1.y == tagA && o1.w == tagB && o2.y == tagA && o2.w == tagB && o3.y == tagA && o3.w == tagB;
            if (__all(ok) || spins > SPIN_CAP) break;
        }
        STAMP(3);
        const float s0 = (p.x + f_of(o1.x)) + (f_of(o2.x) + f_of(o3.x));
        const float s1 = (p.y + f_of(o1.z)) + (f_of(o2.z) + f_of(o3.z));
        const unsigned mine = pack_bf2(s0, s1);
        auto sw = __builtin_amdgcn_permlane32_swap(mine, mine, false, false);
        const unsigned gq = sw[0], uq = sw[1];
        const float h0 = f_of(gq << 16) * f_of(uq << 16), h1 = f_of(gq & 0xffff0000u) * f_of(uq & 0xffff0000u);
        if (lane < 32) ((unsigned*)lds)[512 + lane] = pack_bf2(h0, h1);
        else if (lane < 34) ((unsigned*)lds)[512 + lane] = lane == 32 ? tagA : tagB;
        __syncthreads();
        STAMP(4);
        if (b < 128) {
            const unsigned base = G2_OFF + (unsigned)b * (NS * 64u) + (unsigned)lane * 16u;
            v4u r0, r1, r2, r3;
            for (unsigned spins = 0;; ++spins) {
                asm volatile("" ::: "memory");
                r0 = __builtin_amdgcn_raw_buffer_load_b128(rs, base, 0, 16);
                r1 = __builtin_amdgcn_raw_buffer_load_b128(rs, base + 1024u, 0, 16);
                r2 = __builtin_amdgcn_raw_buffer_load_b128(rs, base + 2048u, 0, 16);
                r3 = __builtin_amdgcn_raw_buffer_load_b128(rs, base + (lane < 32 ? 3072u : 0u), 0, 16);
                const bool ok = r0.y == tagA && r0.w == tagB && r1.y == tagA && r1.w == tagB && r2.y == tagA && r2.w == tagB && r3.y == tagA && r3.w == tagB;
                if (__all(ok) || spins > SPIN_CAP) break;
            }
            STAMP(5);
            float a0 = (f_of(r0.x) + f_of(r1.x)) + (f_of(r2.x) + (lane < 32 ? f_of(r3.x) : 0.f));
            float a1 = (f_of(r0.z) + f_of(r1.z)) + (f_of(r2.z) + (lane < 32 ? f_of(r3.z) : 0.f));
            a0 += dpp<0x124>(a0); a1 += dpp<0x124>(a1);
            a0 += dpp<0x128>(a0); a1 += dpp<0x128>(a1);
            auto sx = __builtin_amdgcn_permlane32_swap(u_of(a0), u_of(a1), false, false);
            float c = f_of(sx[0]) + f_of(sx[1]);
            auto sy = __builtin_amdgcn_permlane16_swap(u_of(c), u_of(c), false, false);
            c = f_of(sy[0]) + f_of(sy[1]);
            auto sz = __builtin_amdgcn_permlane32_swap(u_of(c), u_of(c), false, false);
            if (lane < 4) {
                const int pos = ((lane & 1) << 1) | (lane >> 1);
                __hip_atomic_store((GAS unsigned*)out + b * 4 + pos, pack_bf2(f_of(sz[0]), f_of(sz[1])), RLX_AGENT);
            }
            if (b == 0 && lane == 0) __hip_atomic_store((GAS unsigned long long*)(ws + EP_OFF), ((unsigned long long)tagB << 32) | tagA, RLX_AGENT);
            STAMP(6);
        }
    }
    if (b == 0 && w == 4) { while (__builtin_amdgcn_s_memrealtime() < st[0] + 4000ull) {} }

}

extern "C" void kernel_launch(void* const* d_in, const int* in_sizes, int n_in, void* d_out, int out_size, void* d_ws, size_t ws_size,
                              hipStream_t stream) {
    glu_kernel<<<NWG, 320, 0, stream>>>((const unsigned short*)d_in[0], (const unsigned short*)d_in[1], (const unsigned short*)d_in[2],
                                        (const unsigned short*)d_in[3], (unsigned*)d_out, (unsigned char*)d_ws);
}
```

```cpp
#include <hip/hip_runtime.h>
#include <hip/hip_bf16.h>

typedef unsigned v4u __attribute__((ext_vector_type(4)));
typedef unsigned v2u __attribute__((ext_vector_type(2)));
typedef float v4f __attribute__((ext_vector_type(4)));
typedef float v2f __attribute__((ext_vector_type(2)));
typedef short bf16x8 __attribute__((ext_vector_type(8)));
typedef __bf16 bf2 __attribute__((ext_vector_type(2)));
#define GAS __attribute__((address_space(1)))
#define RLX_AGENT __ATOMIC_RELAXED, __HIP_MEMORY_SCOPE_AGENT

#define NPRE 5
#define PRE0 10
#define STAMPS 0
#define OUT_SC1 1
#define CODEPF 1
#define PREWRITE 3
#define GATE1 -1
#define GATE2 24
#define PSTR 64
#define USEB0 1
#define SLP1 0
#define SLP2 0

constexpr int D = 1024, F = 3584;
constexpr int NS = 56, NPRIM = 224, NWG = 256;
constexpr int AUX_NT = 2, AUX_SC1 = 16;
constexpr unsigned EP_OFF = 0;
constexpr unsigned G1_OFF = 4096;
constexpr unsigned G2_OFF = G1_OFF + NWG * 1024;
constexpr unsigned WS_BYTES = G2_OFF + 128 * NS * PSTR;
constexpr unsigned ST_OFF = 2u << 20;
constexpr unsigned SPIN_CAP = 400000;

typedef __amdgpu_buffer_rsrc_t rsrc_t;
__device__ __forceinline__ rsrc_t mkrs(const void* p, unsigned n) { return __builtin_amdgcn_make_buffer_rsrc((void*)p, 0, n, 0x00020000); }
__device__ __forceinline__ v4f mfma(v4u a, v4u b, v4f c) {
    return __builtin_amdgcn_mfma_f32_16x16x32_bf16(__builtin_bit_cast(bf16x8, a), __builtin_bit_cast(bf16x8, b), c, 0, 0, 0);
}
__device__ __forceinline__ float f_of(unsigned u) { return __builtin_bit_cast(float, u); }
__device__ __forceinline__ unsigned u_of(float f) { return __builtin_bit_cast(unsigned, f); }
__device__ __forceinline__ unsigned pack_bf2(float lo, float hi) { bf2 r; r.x = (__bf16)lo; r.y = (__bf16)hi; return __builtin_bit_cast(unsigned, r); }
template <int CTRL> __device__ __forceinline__ float dpp(float v) {
    return f_of((unsigned)__builtin_amdgcn_update_dpp(0, (int)u_of(v), CTRL, 0xf, 0xf, false));
}
__device__ __forceinline__ unsigned bad(v4u r, unsigned a, unsigned b) { return (r.y ^ a) | (r.w ^ b); }
__device__ __forceinline__ bool none(unsigned v) { return __builtin_amdgcn_ballot_w64(v != 0u) == 0ull; }
#define LD16(rs, vo, so, aux) __builtin_amdgcn_raw_buffer_load_b128(rs, vo, so, aux)
#define ST16(v, rs, vo, so) __builtin_amdgcn_raw_buffer_store_b128(v, rs, vo, so, AUX_SC1)
#define BAR() do { asm volatile("s_waitcnt lgkmcnt(0)" ::: "memory"); __builtin_amdgcn_s_barrier(); asm volatile("" ::: "memory"); } while (0)
#define SMASK 0xffff
#define STAMP(i) do {} while (0)

__global__ void __launch_bounds__(320) __attribute__((aligned(4096)))
glu_kernel(const unsigned short* __restrict__ x, const unsigned short* __restrict__ wg, const unsigned short* __restrict__ wu,
           const unsigned short* __restrict__ wd, unsigned* __restrict__ out, unsigned char* ws) {
    __shared__ __attribute__((aligned(16))) float lds[8 * 64 + 32 + 4];
    STAMP(0);
    unsigned pf = 0;
    if (threadIdx.x < 64 * CODEPF) {
        const unsigned long long pcb = (__builtin_amdgcn_s_getpc() & ~4095ull);
        asm volatile("global_load_dword %0, %1, %2" : "=v"(pf) : "v"(threadIdx.x * 64u), "s"(pcb));
    }
    asm volatile("" :: "s"(x), "s"(wg), "s"(wu), "s"(wd), "s"(out), "s"(ws));
    STAMP(1);
    const int lane = threadIdx.x & 63;
    const int w = __builtin_amdgcn_readfirstlane(threadIdx.x >> 6);
    const int b = blockIdx.x;
    const int k = b & 7, j = b >> 3;
    const bool extra = j >= 28;
    const int slow = (3 - (int)((unsigned)(size_t)wg >> 7) + k) & 7;
    const int sl = extra ? slow : (j >> 2);
    const int q = j & 3;
    const int s = 7 * k + sl;
    const bool half = sl == slow;
    const rsrc_t rs = mkrs(ws, WS_BYTES);
    const int jp = lane & 7, rg = lane >> 3;

    if (__builtin_expect(w < 4, 1)) {
        const int row0 = q * 256 + (half ? (extra ? 128 : 0) + (w & 1) * 64 : (w & 1) * 128);
        const unsigned nrows = sl == 7 ? 0u : (half ? 64u : 128u);
        const rsrc_t rw = mkrs((w < 2 ? wg : wu) + (size_t)row0 * F + s * 64, nrows * (F * 2u));
        const rsrc_t rx = mkrs(x + row0, nrows * 2u);
        const unsigned voff = (unsigned)rg * (8u * F * 2u) + (unsigned)jp * 16u;
        v4u a[16], xa, xb;
        xa = LD16(rx, rg * 16, 0, 0);
        xb = LD16(rx, rg * 16, 128, 0);
        __builtin_amdgcn_sched_barrier(0);
#pragma unroll
        for (int t = 0; t < 8; ++t) a[t] = LD16(rw, voff, t * F * 2, AUX_NT);
#pragma unroll
        for (int t = 8; t < 16; ++t) a[t] = LD16(rw, voff, (56 + t) * F * 2, AUX_NT);
        __builtin_amdgcn_sched_barrier(0);
        const rsrc_t rd = mkrs(wd + (size_t)(s * 64) * D + q * 256 + w * 64, extra ? 0u : 64u * D * 2u);
        const unsigned doff = (unsigned)rg * (8u * D * 2u) + (unsigned)jp * 16u;
        v4u dn[8];
        if (USEB0) __builtin_amdgcn_s_barrier();
        STAMP(2);
        unsigned sel[4][2];
#pragma unroll
        for (int r = 0; r < 4; ++r)
#pragma unroll
            for (int p = 0; p < 2; ++p) {
                const unsigned hsel = p ? 0x0302u : 0x0100u;
                const unsigned on = (jp & 1) ? ((hsel << 16) | 0x0c0cu) : (0x0c0c0000u | hsel);
                sel[r][p] = ((jp >> 1) == r) ? on : 0x0c0c0c0cu;
            }
        v4f acc = {0.f, 0.f, 0.f, 0.f};
#pragma unroll
        for (int t = 0; t < 16; ++t) {
            v4u av;
#pragma unroll
            for (int r = 0; r < 4; ++r) av[r] = __builtin_amdgcn_perm(0u, t < 8 ? xa[t >> 1] : xb[(t - 8) >> 1], sel[r][t & 1]);
            acc = mfma(av, a[t], acc);
            if (t >= PRE0 && t - PRE0 < NPRE) {
                __builtin_amdgcn_sched_barrier(0);
                dn[t - PRE0] = LD16(rd, doff, (t - PRE0) * D * 2, AUX_NT);
                __builtin_amdgcn_sched_barrier(0);
            }
        }
        asm volatile("" :: "v"(pf), "v"(acc));
        if ((lane >> 5) == (rg & 1)) *(v4f*)&lds[(2 * w + (lane >> 5)) * 64 + jp * 8 + 4 * ((lane >> 4) & 1)] = acc;
        STAMP(3);
        BAR();
        if (__builtin_expect(!extra, 1)) {
            const v2u tg = *(const v2u*)&lds[512 + 32];
            BAR();
#pragma unroll
            for (int t = NPRE; t < 8; ++t) dn[t] = LD16(rd, doff, t * D * 2, AUX_NT);
            BAR();
            STAMP(4);
            const v4u hv = *(const v4u*)&lds[512 + 4 * rg];
            v4f o = {0.f, 0.f, 0.f, 0.f};
#pragma unroll
            for (int t = 0; t < 8; ++t) {
                v4u av;
#pragma unroll
                for (int r = 0; r < 4; ++r) av[r] = __builtin_amdgcn_perm(0u, hv[t >> 1], sel[r][t & 1]);
                o = mfma(av, dn[t], o);
            }
            auto s02 = __builtin_amdgcn_permlane32_swap(u_of(o[0]), u_of(o[2]), false, false);
            auto s13 = __builtin_amdgcn_permlane32_swap(u_of(o[1]), u_of(o[3]), false, false);
            const float v0 = f_of(s02[0]) + dpp<0x128>(f_of(s02[1]));
            const float v1 = f_of(s13[0]) + dpp<0x128>(f_of(s13[1]));
            if ((lane & 8) == 0) {
                const v4u gr = {u_of(v0), tg.x, u_of(v1), tg.y};
                ST16(gr, rs, (unsigned)jp * (NS * PSTR) + (unsigned)(lane >> 4) * 16u, G2_OFF + (unsigned)((q * 4 + w) * 8 * NS + s) * PSTR);
            }
            STAMP(5);
        }
    } else {
        const v2u ep = *(const __attribute__((address_space(4))) v2u*)(ws + EP_OFF);
        const unsigned tagA = ep.x + 1u, tagB = ep.y + 1u;
        const unsigned lo = (unsigned)lane * 16u;
        STAMP(2);
        if (lane == 0) *(v2u*)&lds[512 + 32] = (v2u){tagA, tagB};
        if (PREWRITE) {
            const v4u z = {0u, ep.x, 0u, ep.y};
            if (PREWRITE & 1) ST16(z, rs, lo, G1_OFF + (unsigned)b * 1024u);
            if ((PREWRITE & 2) && !extra) {
                constexpr int LPP = PSTR / 16;
#pragma unroll
                for (int i = 0; i < 32 * LPP / 64; ++i) {
                    const unsigned gi = i * 64 + lane;
                    ST16(z, rs, (gi / LPP) * (NS * PSTR) + (gi % LPP) * 16u, G2_OFF + (unsigned)(q * 32 * NS + s) * PSTR);
                }
            }
            if (USEB0) __builtin_amdgcn_s_barrier();
            asm volatile("s_waitcnt vmcnt(0)" ::: "memory");
        } else if (USEB0) __builtin_amdgcn_s_barrier();
        BAR();
        STAMP(3);
        const float* rp = &lds[(lane >> 5) * 256 + 2 * (lane & 31)];
        const v2f p = (*(const v2f*)rp + *(const v2f*)(rp + 64)) + (*(const v2f*)(rp + 128) + *(const v2f*)(rp + 192));
        {
            const v4u gr = {u_of(p.x), tagA, u_of(p.y), tagB};
            ST16(gr, rs, lo, G1_OFF + (unsigned)b * 1024u);
        }
        STAMP(4);
        if (!extra) {
            BAR();
            if (GATE1 < 0) asm volatile("s_waitcnt vmcnt(0)" ::: "memory");
            else if (GATE1 > 0) __builtin_amdgcn_s_sleep(GATE1);
            STAMP(8);
            float s0, s1;
            {
                v4u o1, o2, o3;
                for (unsigned spins = 0;; ++spins) {
                    asm volatile("" ::: "memory");
                    o1 = LD16(rs, lo, G1_OFF + (unsigned)(b ^ 8) * 1024u, AUX_SC1);
                    o2 = LD16(rs, lo, G1_OFF + (unsigned)(b ^ 16) * 1024u, AUX_SC1);
                    o3 = LD16(rs, lo, G1_OFF + (unsigned)(b ^ 24) * 1024u, AUX_SC1);
                    if (none(bad(o1, tagA, tagB) | bad(o2, tagA, tagB) | bad(o3, tagA, tagB)) || spins > SPIN_CAP) break;
                    if (SLP1) __builtin_amdgcn_s_sleep(SLP1);
                }
                s0 = (p.x + f_of(o1.x)) + (f_of(o2.x) + f_of(o3.x));
                s1 = (p.y + f_of(o1.z)) + (f_of(o2.z) + f_of(o3.z));
            }
            if (half) {
                v4u o0, o1, o2, o3;
                const unsigned eb = G1_OFF + (unsigned)(NPRIM + k) * 1024u;
                for (unsigned spins = 0;; ++spins) {
                    asm volatile("" ::: "memory");
                    o0 = LD16(rs, lo, eb, AUX_SC1);
                    o1 = LD16(rs, lo, eb + 8192u, AUX_SC1);
                    o2 = LD16(rs, lo, eb + 16384u, AUX_SC1);
                    o3 = LD16(rs, lo, eb + 24576u, AUX_SC1);
                    if (none(bad(o0, tagA, tagB) | bad(o1, tagA, tagB) | bad(o2, tagA, tagB) | bad(o3, tagA, tagB)) || spins > SPIN_CAP) break;
                }
                s0 += (f_of(o0.x) + f_of(o1.x)) + (f_of(o2.x) + f_of(o3.x));
                s1 += (f_of(o0.z) + f_of(o1.z)) + (f_of(o2.z) + f_of(o3.z));
            }
            STAMP(5);
            const unsigned mine = pack_bf2(s0, s1);
            auto sw = __builtin_amdgcn_permlane32_swap(mine, mine, false, false);
            const unsigned gq = sw[0], uq = sw[1];
            const float h0 = f_of(gq << 16) * f_of(uq << 16), h1 = f_of(gq & 0xffff0000u) * f_of(uq & 0xffff0000u);
            if (lane < 32) ((unsigned*)lds)[512 + lane] = pack_bf2(h0, h1);
            BAR();
            if (b < 128) {
                const unsigned base = G2_OFF + (unsigned)b * (NS * PSTR);
                const unsigned o0 = (unsigned)(lane >> 2) * PSTR + (unsigned)(lane & 3) * 16u, o3 = o0 + (lane < 32 ? 48u * PSTR : 0u);
                if (GATE2) __builtin_amdgcn_s_sleep(GATE2);
                STAMP(9);
                v4u r0, r1, r2, r3;
                for (unsigned spins = 0;; ++spins) {
                    asm volatile("" ::: "memory");
                    r0 = LD16(rs, o0, base, AUX_SC1);
                    r1 = LD16(rs, o0, base + 16u * PSTR, AUX_SC1);
                    r2 = LD16(rs, o0, base + 32u * PSTR, AUX_SC1);
                    r3 = LD16(rs, o3, base, AUX_SC1);
                    if (none(bad(r0, tagA, tagB) | bad(r1, tagA, tagB) | bad(r2, tagA, tagB) | bad(r3, tagA, tagB)) || spins > SPIN_CAP) break;
                    if (SLP2) __builtin_amdgcn_s_sleep(SLP2);
                }
                STAMP(6);
                float a0 = (f_of(r0.x) + f_of(r1.x)) + (f_of(r2.x) + (lane < 32 ? f_of(r3.x) : 0.f));
                float a1 = (f_of(r0.z) + f_of(r1.z)) + (f_of(r2.z) + (lane < 32 ? f_of(r3.z) : 0.f));
                a0 += dpp<0x124>(a0); a1 += dpp<0x124>(a1);
                a0 += dpp<0x128>(a0); a1 += dpp<0x128>(a1);
                auto sx = __builtin_amdgcn_permlane32_swap(u_of(a0), u_of(a1), false, false);
                float cc = f_of(sx[0]) + f_of(sx[1]);
                auto sy = __builtin_amdgcn_permlane16_swap(u_of(cc), u_of(cc), false, false);
                cc = f_of(sy[0]) + f_of(sy[1]);
                auto sz = __builtin_amdgcn_permlane32_swap(u_of(cc), u_of(cc), false, false);
                if (lane < 4) {
                    const int pos = ((lane & 1) << 1) | (lane >> 1);
                    __hip_atomic_store((GAS unsigned*)out + b * 4 + pos, pack_bf2(f_of(sz[0]), f_of(sz[1])), RLX_AGENT);
                }
                if (b == 0 && lane == 0) __builtin_amdgcn_raw_buffer_store_b64((v2u){tagA, tagB}, rs, 0, EP_OFF, AUX_SC1);
                STAMP(7);
            }
        }
    }
}

extern "C" void kernel_launch(void* const* d_in, const int* in_sizes, int n_in, void* d_out, int out_size, void* d_ws, size_t ws_size,
                              hipStream_t stream) {
    glu_kernel<<<NWG, 320, 0, stream>>>((const unsigned short*)d_in[0], (const unsigned short*)d_in[1], (const unsigned short*)d_in[2],
                                        (const unsigned short*)d_in[3], (unsigned*)d_out, (unsigned char*)d_ws);
}
```

```cpp
#include <hip/hip_runtime.h>
#include <hip/hip_bf16.h>

typedef unsigned v4u __attribute__((ext_vector_type(4)));
typedef unsigned v8u __attribute__((ext_vector_type(8)));
typedef unsigned v2u __attribute__((ext_vector_type(2)));
typedef float v4f __attribute__((ext_vector_type(4)));
typedef float v2f __attribute__((ext_vector_type(2)));
typedef short bf16x8 __attribute__((ext_vector_type(8)));
typedef __bf16 bf2 __attribute__((ext_vector_type(2)));
#define GAS __attribute__((address_space(1)))

#define NPRE 6
#define PRE0 10
#define STAMPS 0
#define OUT_AUX 0
#define GATE1 -1
#define GATE2 24
#define USEB0 1
#define SLP1 0
#define SLP2 0
#define ABL 0
#define XAUX 0
#define NHEAD 0
#define GABS 0
#define RPAD 128
#define CMAP 1
#define XPOS 4

constexpr int D = 1024, F = 3584;
constexpr int NS = 56, NPRIM = 224, NWG = 256;
constexpr int AUX_NT = 2, AUX_SC1 = 16;
constexpr unsigned EP_OFF = 0;
constexpr unsigned G1_OFF = 4096;
constexpr unsigned G2_OFF = G1_OFF + NWG * 512;
constexpr unsigned RS2 = 4096 + RPAD;
constexpr unsigned WS_BYTES = G2_OFF + NS * RS2;
constexpr unsigned ST_OFF = 2u << 20;
constexpr unsigned SPIN_CAP = 400000;

typedef __amdgpu_buffer_rsrc_t rsrc_t;
__device__ __forceinline__ rsrc_t mkrs(const void* p, unsigned n) { return __builtin_amdgcn_make_buffer_rsrc((void*)p, 0, n, 0x00020000); }
__device__ __forceinline__ v4f mfma(v4u a, v4u b, v4f c) {
    return __builtin_amdgcn_mfma_f32_16x16x32_bf16(__builtin_bit_cast(bf16x8, a), __builtin_bit_cast(bf16x8, b), c, 0, 0, 0);
}
__device__ __forceinline__ float f_of(unsigned u) { return __builtin_bit_cast(float, u); }
__device__ __forceinline__ unsigned u_of(float f) { return __builtin_bit_cast(unsigned, f); }
__device__ __forceinline__ unsigned pack_bf2(float lo, float hi) { bf2 r; r.x = (__bf16)lo; r.y = (__bf16)hi; return __builtin_bit_cast(unsigned, r); }
template <int CTRL> __device__ __forceinline__ float dpp(float v) {
    return f_of((unsigned)__builtin_amdgcn_update_dpp(0, (int)u_of(v), CTRL, 0xf, 0xf, true));
}
__device__ __forceinline__ unsigned enc(float f, unsigned t) { return ((u_of(f) + 0x80u) & ~0xffu) | (t & 0xffu); }
__device__ __forceinline__ float dec(unsigned u) { return f_of(u & ~0xffu); }
__device__ __forceinline__ bool none(unsigned v) { return __builtin_amdgcn_ballot_w64((v & 0xffu) != 0u) == 0ull; }
#define LD16(rs, vo, so, aux) __builtin_amdgcn_raw_buffer_load_b128(rs, vo, so, aux)
#define LD8(rs, vo, so, aux) __builtin_amdgcn_raw_buffer_load_b64(rs, vo, so, aux)
#define ST16(v, rs, vo, so) __builtin_amdgcn_raw_buffer_store_b128(v, rs, vo, so, AUX_SC1)
#define ST8(v, rs, vo, so) __builtin_amdgcn_raw_buffer_store_b64(v, rs, vo, so, AUX_SC1)
#define BAR() do { asm volatile("s_waitcnt lgkmcnt(0)" ::: "memory"); __builtin_amdgcn_s_barrier(); asm volatile("" ::: "memory"); } while (0)
#define SMASK 0xffff
#define STAMP(i) do {} while (0)

__global__ void __launch_bounds__(320) __attribute__((aligned(4096)))
glu_kernel(const unsigned short* x_, const unsigned short* wg_, const unsigned short* wu_, const unsigned short* wd_, unsigned short* out_, unsigned char* ws_) {
    __shared__ __attribute__((aligned(16))) float lds[8 * 64 + 32 + 4];
    v8u ka; v4u kb;
    asm volatile("s_load_dwordx8 %0, %2, 0x0\n\ts_load_dwordx4 %1, %2, 0x20" : "=&s"(ka), "=&s"(kb) : "s"(__builtin_amdgcn_kernarg_segment_ptr()));
#define KWAIT() asm volatile("s_waitcnt lgkmcnt(0)" : "+s"(ka), "+s"(kb))
#define PTR(lo, hi) ((void*)(((unsigned long long)(hi) << 32) | (lo)))
    STAMP(0);
    unsigned pf = 0;
    if (!(ABL & 1) && threadIdx.x < 36) {
        const unsigned long long pcb = (__builtin_amdgcn_s_getpc() & ~4095ull);
        asm volatile("global_load_dword %0, %1, %2" : "=v"(pf) : "v"(threadIdx.x * 128u), "s"(pcb));
    }
    const int lane = threadIdx.x & 63;
    const int w = __builtin_amdgcn_readfirstlane(threadIdx.x >> 6);
    const int b = blockIdx.x;
    const int k = b & 7, j = b >> 3;
    const bool extra = j >= 28;
    const int slow = (3 + k) & 7;
    const int sl = extra ? slow : (j >> 2);
    const int q = j & 3;
    const int s = 7 * k + sl;
    const bool half = sl == slow;
    const int jp = lane & 7, rg = lane >> 3;

    if (__builtin_expect(w < 4, 1)) {
        const unsigned row0 = q * 256 + (extra ? 128 : 0) + ((w & 1) << (half ? 6 : 7));
        const unsigned lim = sl == 7 ? 0u : (row0 + (half ? 64u : 128u)) * (F * 2u);
        const unsigned voff = (row0 + 8u * rg) * (F * 2u) + (unsigned)s * 128u + (unsigned)jp * 16u;
        const unsigned xoff = row0 * 2u + (unsigned)rg * 16u;
        const unsigned doff = (unsigned)(s * 64 + 8 * rg) * (D * 2u) + (unsigned)(q * 512 + w * 128 + jp * 16);
        unsigned sel[4][2];
#pragma unroll
        for (int r = 0; r < 4; ++r)
#pragma unroll
            for (int p = 0; p < 2; ++p) {
                const unsigned hsel = p ? 0x0302u : 0x0100u;
                const unsigned on = (jp & 1) ? ((hsel << 16) | 0x0c0cu) : (0x0c0c0000u | hsel);
                sel[r][p] = ((jp >> 1) == r) ? on : 0x0c0c0c0cu;
            }
        asm volatile("" :: "v"(voff), "v"(xoff), "v"(doff), "s"(lim));
        __builtin_amdgcn_sched_barrier(0);
        KWAIT();
        STAMP(1);
        const rsrc_t rw = mkrs(PTR(w < 2 ? ka[2] : ka[4], w < 2 ? ka[3] : ka[5]), lim);
        const rsrc_t rx = mkrs(PTR(ka[0], ka[1]), D * 2u);
        v4u a[16], xa, xb, dn[8];
        const rsrc_t rd = mkrs(PTR(ka[6], ka[7]), extra ? 0u : (unsigned)F * D * 2u);
#pragma unroll
        for (int t = 0; t < NHEAD; ++t) dn[t] = LD16(rd, doff, t * D * 2, AUX_NT);
        __builtin_amdgcn_sched_barrier(0);
#pragma unroll
        for (int t = 0; t < 16; ++t) {
            if (t == XPOS) { __builtin_amdgcn_sched_barrier(0); if (ABL & 2) { xa = (v4u){xoff, xoff, voff, doff}; xb = xa; } else { xa = LD16(rx, xoff, 0, XAUX); xb = LD16(rx, xoff, 128, XAUX); } __builtin_amdgcn_sched_barrier(0); }
            a[t] = LD16(rw, voff, (t < 8 ? t : 56 + t) * F * 2, AUX_NT);
        }
        __builtin_amdgcn_sched_barrier(0);
        const rsrc_t rs = mkrs(PTR(kb[2], kb[3]), WS_BYTES);
        if (USEB0) __builtin_amdgcn_s_barrier();
        STAMP(2);
        v4f acc = {0.f, 0.f, 0.f, 0.f};
#pragma unroll
        for (int t = 0; t < 16; ++t) {
            v4u av;
#pragma unroll
            for (int r = 0; r < 4; ++r) av[r] = __builtin_amdgcn_perm(0u, t < 8 ? xa[t >> 1] : xb[(t - 8) >> 1], sel[r][t & 1]);
            acc = mfma(av, a[t], acc);
            if (t >= PRE0 && NHEAD + t - PRE0 < NPRE) {
                __builtin_amdgcn_sched_barrier(0);
                dn[NHEAD + t - PRE0] = LD16(rd, doff, (NHEAD + t - PRE0) * D * 2, AUX_NT);
                __builtin_amdgcn_sched_barrier(0);
            }
        }
        asm volatile("" :: "v"(pf), "v"(acc));
        if ((lane >> 5) == (rg & 1)) *(v4f*)&lds[(2 * w + (lane >> 5)) * 64 + jp * 8 + 4 * ((lane >> 4) & 1)] = acc;
        STAMP(3);
        BAR();
        if (__builtin_expect(!extra, 1)) {
            const int g4 = lane >> 4, pos = ((g4 & 1) << 1) | (g4 >> 1);
            const unsigned tw = ((const unsigned*)lds)[512 + 32 + (pos >> 1)] >> (16 * (pos & 1));
            const unsigned goff = (lane & 8) ? WS_BYTES : (unsigned)jp * 32u + (unsigned)pos * 8u;
            if (NPRE < 8) {
                BAR();
#pragma unroll
                for (int t = NPRE; t < 8; ++t) dn[t] = LD16(rd, doff, t * D * 2, AUX_NT);
            }
            asm volatile("" :: "v"(tw), "v"(goff));
            BAR();
            STAMP(4);
            const v4u hv = *(const v4u*)&lds[512 + 4 * rg];
            v4f o = {0.f, 0.f, 0.f, 0.f};
#pragma unroll
            for (int t = 0; t < 8; ++t) {
                v4u av;
#pragma unroll
                for (int r = 0; r < 4; ++r) av[r] = __builtin_amdgcn_perm(0u, hv[t >> 1], sel[r][t & 1]);
                o = mfma(av, dn[t], o);
            }
            auto s02 = __builtin_amdgcn_permlane32_swap(u_of(o[0]), u_of(o[2]), false, false);
            auto s13 = __builtin_amdgcn_permlane32_swap(u_of(o[1]), u_of(o[3]), false, false);
            const float v0 = f_of(s02[0]) + dpp<0x128>(f_of(s02[1]));
            const float v1 = f_of(s13[0]) + dpp<0x128>(f_of(s13[1]));
            const v2u gr = {enc(v0, tw), enc(v1, tw >> 8)};
            ST8(gr, rs, goff, G2_OFF + (unsigned)s * RS2 + (unsigned)(q * 4 + w) * 256u);
            STAMP(5);
        }
    } else {
        const unsigned long long tstart = GABS ? __builtin_amdgcn_s_memrealtime() : 0ull;
        asm volatile("s_waitcnt lgkmcnt(0)" :: "s"(ka), "s"(kb));
        const rsrc_t rs = mkrs(ws_, WS_BYTES);
        const v2u ep = *(const __attribute__((address_space(4))) v2u*)ws_;
        const unsigned tagA = ((ep.x & 0x7f7f7f7fu) + 0x01010101u) ^ (ep.x & 0x80808080u);
        const unsigned tagB = ((ep.y & 0x7f7f7f7fu) + 0x01010101u) ^ (ep.y & 0x80808080u);
        const unsigned lo = (unsigned)lane * 8u;
        const unsigned t2 = ((lane & 2) ? tagB : tagA) >> (16 * (lane & 1));
        const unsigned t4 = (lane & 1) ? tagB : tagA;
        STAMP(2);
        if (lane == 0) *(v2u*)&lds[512 + 32] = (v2u){tagA, tagB};
        {
            if (!(ABL & 4)) ST8(((v2u){~t2, ~(t2 >> 8)}), rs, lo, G1_OFF + (unsigned)b * 512u);
            const v4u z = {~t4, ~(t4 >> 8), ~(t4 >> 16), ~(t4 >> 24)};
            if (!(ABL & 4)) ST16(z, rs, (unsigned)lane * 16u, extra ? WS_BYTES : G2_OFF + (unsigned)s * RS2 + (unsigned)q * 1024u);
            if (USEB0) __builtin_amdgcn_s_barrier();
            asm volatile("s_waitcnt vmcnt(0)" ::: "memory");
        }
        const float* rp = &lds[(lane >> 5) * 256 + 2 * (lane & 31)];
        BAR();
        STAMP(3);
        const v2f p = (*(const v2f*)rp + *(const v2f*)(rp + 64)) + (*(const v2f*)(rp + 128) + *(const v2f*)(rp + 192));
        const v2u mine = {enc(p.x, t2), enc(p.y, t2 >> 8)};
        ST8(mine, rs, lo, G1_OFF + (unsigned)b * 512u);
        STAMP(4);
        if (!extra) {
            if (NPRE < 8) BAR();
            if (GATE1 < 0) asm volatile("s_waitcnt vmcnt(0)" ::: "memory");
            else if (GATE1 > 0) __builtin_amdgcn_s_sleep(GATE1);
            STAMP(8);
            const unsigned t2h = t2 >> 8;
            float s0, s1;
            const unsigned p1 = G1_OFF + (unsigned)(b ^ 8) * 512u, p2 = G1_OFF + (unsigned)(b ^ 16) * 512u, p3 = G1_OFF + (unsigned)(b ^ 24) * 512u;
            if (__builtin_expect(!half, 1)) {
                v2u o1, o2, o3;
                for (unsigned spins = 0;; ++spins) {
                    asm volatile("" ::: "memory");
                    o1 = LD8(rs, lo, p1, AUX_SC1); o2 = LD8(rs, lo, p2, AUX_SC1); o3 = LD8(rs, lo, p3, AUX_SC1);
                    if (none((o1.x ^ t2) | (o1.y ^ t2h) | (o2.x ^ t2) | (o2.y ^ t2h) | (o3.x ^ t2) | (o3.y ^ t2h)) || spins > SPIN_CAP) break;
                    if (SLP1) __builtin_amdgcn_s_sleep(SLP1);
                }
                s0 = (dec(mine.x) + dec(o1.x)) + (dec(o2.x) + dec(o3.x));
                s1 = (dec(mine.y) + dec(o1.y)) + (dec(o2.y) + dec(o3.y));
            } else {
                v2u o1, o2, o3, e0, e1, e2, e3;
                const unsigned eb = G1_OFF + (unsigned)(NPRIM + k) * 512u;
                for (unsigned spins = 0;; ++spins) {
                    asm volatile("" ::: "memory");
                    o1 = LD8(rs, lo, p1, AUX_SC1); o2 = LD8(rs, lo, p2, AUX_SC1); o3 = LD8(rs, lo, p3, AUX_SC1);
                    e0 = LD8(rs, lo, eb, AUX_SC1); e1 = LD8(rs, lo, eb + 4096u, AUX_SC1); e2 = LD8(rs, lo, eb + 8192u, AUX_SC1); e3 = LD8(rs, lo, eb + 12288u, AUX_SC1);
                    if (none((o1.x ^ t2) | (o1.y ^ t2h) | (o2.x ^ t2) | (o2.y ^ t2h) | (o3.x ^ t2) | (o3.y ^ t2h) |
                             (e0.x ^ t2) | (e0.y ^ t2h) | (e1.x ^ t2) | (e1.y ^ t2h) | (e2.x ^ t2) | (e2.y ^ t2h) | (e3.x ^ t2) | (e3.y ^ t2h)) || spins > SPIN_CAP) break;
                    if (SLP1) __builtin_amdgcn_s_sleep(SLP1);
                }
                s0 = ((dec(mine.x) + dec(o1.x)) + (dec(o2.x) + dec(o3.x))) + ((dec(e0.x) + dec(e1.x)) + (dec(e2.x) + dec(e3.x)));
                s1 = ((dec(mine.y) + dec(o1.y)) + (dec(o2.y) + dec(o3.y))) + ((dec(e0.y) + dec(e1.y)) + (dec(e2.y) + dec(e3.y)));
            }
            STAMP(5);
            const unsigned gu = pack_bf2(s0, s1);
            auto sw = __builtin_amdgcn_permlane32_swap(gu, gu, false, false);
            const unsigned gq = sw[0], uq = sw[1];
            const float h0 = f_of(gq << 16) * f_of(uq << 16), h1 = f_of(gq & 0xffff0000u) * f_of(uq & 0xffff0000u);
            ((unsigned*)lds)[512 + (lane & 31)] = pack_bf2(h0, h1);
            BAR();
            if (b < 128) {
                const unsigned c = CMAP ? 4u * (unsigned)(k + 8 * (b >> 5)) + (unsigned)q : (unsigned)b;
                const unsigned base = G2_OFF + c * 32u;
                const unsigned o0 = (unsigned)(lane >> 1) * RS2 + (unsigned)(lane & 1) * 16u, o1 = o0 + (lane < 48 ? 32u * RS2 : 0u);
                const unsigned ta = t4, tb = t4 >> 8, tc = t4 >> 16, td = t4 >> 24;
                const rsrc_t ro = mkrs(out_, D * 2u);
                const unsigned ooff = (lane & 14) ? D * 2u : (unsigned)(4 * (lane & 1) + (lane >> 5) + 2 * ((lane >> 4) & 1)) * 2u;
                if (GATE2) __builtin_amdgcn_s_sleep(GATE2);
                if (GABS) while (__builtin_amdgcn_s_memrealtime() < tstart + GABS) {}
                STAMP(9);
                v4u r0, r1;
                for (unsigned spins = 0;; ++spins) {
                    asm volatile("" ::: "memory");
                    r0 = LD16(rs, o0, base, AUX_SC1);
                    r1 = LD16(rs, o1, base, AUX_SC1);
                    if (none((r0.x ^ ta) | (r0.y ^ tb) | (r0.z ^ tc) | (r0.w ^ td) | (r1.x ^ ta) | (r1.y ^ tb) | (r1.z ^ tc) | (r1.w ^ td)) || spins > SPIN_CAP) break;
                    if (SLP2) __builtin_amdgcn_s_sleep(SLP2);
                }
                STAMP(6);
                const unsigned m1 = lane < 48 ? ~0xffu : 0u;
                const float a0 = dec(r0.x) + f_of(r1.x & m1), a1 = dec(r0.y) + f_of(r1.y & m1), a2 = dec(r0.z) + f_of(r1.z & m1), a3 = dec(r0.w) + f_of(r1.w & m1);
                auto s01 = __builtin_amdgcn_permlane32_swap(u_of(a0), u_of(a1), false, false);
                auto s23 = __builtin_amdgcn_permlane32_swap(u_of(a2), u_of(a3), false, false);
                const float c01 = f_of(s01[0]) + f_of(s01[1]), c23 = f_of(s23[0]) + f_of(s23[1]);
                auto sy = __builtin_amdgcn_permlane16_swap(u_of(c01), u_of(c23), false, false);
                float cc = f_of(sy[0]) + f_of(sy[1]);
                cc += dpp<0x128>(cc); cc += dpp<0x124>(cc); cc += dpp<0x122>(cc);
                __builtin_amdgcn_raw_buffer_store_b16((unsigned short)pack_bf2(cc, cc), ro, ooff, c * 16u, OUT_AUX);
                if (b == 0 && lane == 0) ST8(((v2u){tagA, tagB}), rs, 0, EP_OFF);
                STAMP(7);
            }
        }
    }
}

extern "C" void kernel_launch(void* const* d_in, const int* in_sizes, int n_in, void* d_out, int out_size, void* d_ws, size_t ws_size,
                              hipStream_t stream) {
    glu_kernel<<<NWG, 320, 0, stream>>>((const unsigned short*)d_in[0], (const unsigned short*)d_in[1], (const unsigned short*)d_in[2],
                                        (const unsigned short*)d_in[3], (unsigned short*)d_out, (unsigned char*)d_ws);
}
```

```cpp
#include <hip/hip_runtime.h>
#include <hip/hip_bf16.h>
#include <cstdio>

typedef unsigned v4u __attribute__((ext_vector_type(4)));
typedef unsigned v2u __attribute__((ext_vector_type(2)));
typedef float v4f __attribute__((ext_vector_type(4)));
typedef float v2f __attribute__((ext_vector_type(2)));
typedef short bf16x8 __attribute__((ext_vector_type(8)));
typedef __bf16 bf2 __attribute__((ext_vector_type(2)));
#define GAS __attribute__((address_space(1)))
#define RLX_AGENT __ATOMIC_RELAXED, __HIP_MEMORY_SCOPE_AGENT

#define NPRE 5
#define PRE0 10
#define STAMPS 0
#define OUT_SC1 1
#define CODEPF 1
#define PREWRITE 3
#define GATE1 -1
#define GATE2 24
#define PSTR 64
#define USEB0 1
#define SLP1 0
#define SLP2 0

constexpr int D = 1024, F = 3584;
constexpr int NS = 56, NPRIM = 224, NWG = 256;
constexpr int AUX_NT = 2, AUX_SC1 = 16;
constexpr unsigned EP_OFF = 0;
constexpr unsigned G1_OFF = 4096;
constexpr unsigned G2_OFF = G1_OFF + NWG * 1024;
constexpr unsigned WS_BYTES = G2_OFF + 128 * NS * PSTR;
constexpr unsigned ST_OFF = 2u << 20;
constexpr unsigned SPIN_CAP = 400000;

typedef __amdgpu_buffer_rsrc_t rsrc_t;
__device__ __forceinline__ rsrc_t mkrs(const void* p, unsigned n) { return __builtin_amdgcn_make_buffer_rsrc((void*)p, 0, n, 0x00020000); }
__device__ __forceinline__ v4f mfma(v4u a, v4u b, v4f c) {
    return __builtin_amdgcn_mfma_f32_16x16x32_bf16(__builtin_bit_cast(bf16x8, a), __builtin_bit_cast(bf16x8, b), c, 0, 0, 0);
}
__device__ __forceinline__ float f_of(unsigned u) { return __builtin_bit_cast(float, u); }
__device__ __forceinline__ unsigned u_of(float f) { return __builtin_bit_cast(unsigned, f); }
__device__ __forceinline__ unsigned pack_bf2(float lo, float hi) { bf2 r; r.x = (__bf16)lo; r.y = (__bf16)hi; return __builtin_bit_cast(unsigned, r); }
template <int CTRL> __device__ __forceinline__ float dpp(float v) {
    return f_of((unsigned)__builtin_amdgcn_update_dpp(0, (int)u_of(v), CTRL, 0xf, 0xf, false));
}
__device__ __forceinline__ unsigned bad(v4u r, unsigned a, unsigned b) { return (r.y ^ a) | (r.w ^ b); }
__device__ __forceinline__ bool none(unsigned v) { return __builtin_amdgcn_ballot_w64(v != 0u) == 0ull; }
#define LD16(rs, vo, so, aux) __builtin_amdgcn_raw_buffer_load_b128(rs, vo, so, aux)
#define ST16(v, rs, vo, so) __builtin_amdgcn_raw_buffer_store_b128(v, rs, vo, so, AUX_SC1)
#define BAR() do { asm volatile("s_waitcnt lgkmcnt(0)" ::: "memory"); __builtin_amdgcn_s_barrier(); asm volatile("" ::: "memory"); } while (0)
#define SMASK 0xffff
#define STAMP(i) do {} while (0)

__global__ void __launch_bounds__(320) __attribute__((aligned(4096)))
glu_kernel(const unsigned short* __restrict__ x, const unsigned short* __restrict__ wg, const unsigned short* __restrict__ wu,
           const unsigned short* __restrict__ wd, unsigned* __restrict__ out, unsigned char* ws) {
    __shared__ __attribute__((aligned(16))) float lds[8 * 64 + 32 + 4];
    STAMP(0);
    unsigned pf = 0;
    if (threadIdx.x < 64 * CODEPF) {
        const unsigned long long pcb = (__builtin_amdgcn_s_getpc() & ~4095ull);
        asm volatile("global_load_dword %0, %1, %2" : "=v"(pf) : "v"(threadIdx.x * 64u), "s"(pcb));
    }
    asm volatile("" :: "s"(x), "s"(wg), "s"(wu), "s"(wd), "s"(out), "s"(ws));
    STAMP(1);
    const int lane = threadIdx.x & 63;
    const int w = __builtin_amdgcn_readfirstlane(threadIdx.x >> 6);
    const int b = blockIdx.x;
    const int k = b & 7, j = b >> 3;
    const bool extra = j >= 28;
    const int slow = (3 - (int)((unsigned)(size_t)wg >> 7) + k) & 7;
    const int sl = extra ? slow : (j >> 2);
    const int q = j & 3;
    const int s = 7 * k + sl;
    const bool half = sl == slow;
    const rsrc_t rs = mkrs(ws, WS_BYTES);
    const int jp = lane & 7, rg = lane >> 3;

    if (__builtin_expect(w < 4, 1)) {
        const int row0 = q * 256 + (half ? (extra ? 128 : 0) + (w & 1) * 64 : (w & 1) * 128);
        const unsigned nrows = sl == 7 ? 0u : (half ? 64u : 128u);
        const rsrc_t rw = mkrs((w < 2 ? wg : wu) + (size_t)row0 * F + s * 64, nrows * (F * 2u));
        const rsrc_t rx = mkrs(x + row0, nrows * 2u);
        const unsigned voff = (unsigned)rg * (8u * F * 2u) + (unsigned)jp * 16u;
        v4u a[16], xa, xb;
        xa = LD16(rx, rg * 16, 0, 0);
        xb = LD16(rx, rg * 16, 128, 0);
        __builtin_amdgcn_sched_barrier(0);
#pragma unroll
        for (int t = 0; t < 8; ++t) a[t] = LD16(rw, voff, t * F * 2, AUX_NT);
#pragma unroll
        for (int t = 8; t < 16; ++t) a[t] = LD16(rw, voff, (56 + t) * F * 2, AUX_NT);
        __builtin_amdgcn_sched_barrier(0);
        const rsrc_t rd = mkrs(wd + (size_t)(s * 64) * D + q * 256 + w * 64, extra ? 0u : 64u * D * 2u);
        const unsigned doff = (unsigned)rg * (8u * D * 2u) + (unsigned)jp * 16u;
        v4u dn[8];
        if (USEB0) __builtin_amdgcn_s_barrier();
        STAMP(2);
        unsigned sel[4][2];
#pragma unroll
        for (int r = 0; r < 4; ++r)
#pragma unroll
            for (int p = 0; p < 2; ++p) {
                const unsigned hsel = p ? 0x0302u : 0x0100u;
                const unsigned on = (jp & 1) ? ((hsel << 16) | 0x0c0cu) : (0x0c0c0000u | hsel);
                sel[r][p] = ((jp >> 1) == r) ? on : 0x0c0c0c0cu;
            }
        v4f acc = {0.f, 0.f, 0.f, 0.f};
#pragma unroll
        for (int t = 0; t < 16; ++t) {
            v4u av;
#pragma unroll
            for (int r = 0; r < 4; ++r) av[r] = __builtin_amdgcn_perm(0u, t < 8 ? xa[t >> 1] : xb[(t - 8) >> 1], sel[r][t & 1]);
            acc = mfma(av, a[t], acc);
            if (t >= PRE0 && t - PRE0 < NPRE) {
                __builtin_amdgcn_sched_barrier(0);
                dn[t - PRE0] = LD16(rd, doff, (t - PRE0) * D * 2, AUX_NT);
                __builtin_amdgcn_sched_barrier(0);
            }
        }
        asm volatile("" :: "v"(pf), "v"(acc));
        if ((lane >> 5) == (rg & 1)) *(v4f*)&lds[(2 * w + (lane >> 5)) * 64 + jp * 8 + 4 * ((lane >> 4) & 1)] = acc;
        STAMP(3);
        BAR();
        if (__builtin_expect(!extra, 1)) {
            const v2u tg = *(const v2u*)&lds[512 + 32];
            BAR();
#pragma unroll
            for (int t = NPRE; t < 8; ++t) dn[t] = LD16(rd, doff, t * D * 2, AUX_NT);
            BAR();
            STAMP(4);
            const v4u hv = *(const v4u*)&lds[512 + 4 * rg];
            v4f o = {0.f, 0.f, 0.f, 0.f};
#pragma unroll
            for (int t = 0; t < 8; ++t) {
                v4u av;
#pragma unroll
                for (int r = 0; r < 4; ++r) av[r] = __builtin_amdgcn_perm(0u, hv[t >> 1], sel[r][t & 1]);
                o = mfma(av, dn[t], o);
            }
            auto s02 = __builtin_amdgcn_permlane32_swap(u_of(o[0]), u_of(o[2]), false, false);
            auto s13 = __builtin_amdgcn_permlane32_swap(u_of(o[1]), u_of(o[3]), false, false);
            const float v0 = f_of(s02[0]) + dpp<0x128>(f_of(s02[1]));
            const float v1 = f_of(s13[0]) + dpp<0x128>(f_of(s13[1]));
            if ((lane & 8) == 0) {
                const v4u gr = {u_of(v0), tg.x, u_of(v1), tg.y};
                ST16(gr, rs, (unsigned)jp * (NS * PSTR) + (unsigned)(lane >> 4) * 16u, G2_OFF + (unsigned)((q * 4 + w) * 8 * NS + s) * PSTR);
            }
            STAMP(5);
        }
    } else {
        const v2u ep = *(const __attribute__((address_space(4))) v2u*)(ws + EP_OFF);
        const unsigned tagA = ep.x + 1u, tagB = ep.y + 1u;
        const unsigned lo = (unsigned)lane * 16u;
        STAMP(2);
        if (lane == 0) *(v2u*)&lds[512 + 32] = (v2u){tagA, tagB};
        if (PREWRITE) {
            const v4u z = {0u, ep.x, 0u, ep.y};
            if (PREWRITE & 1) ST16(z, rs, lo, G1_OFF + (unsigned)b * 1024u);
            if ((PREWRITE & 2) && !extra) {
                constexpr int LPP = PSTR / 16;
#pragma unroll
                for (int i = 0; i < 32 * LPP / 64; ++i) {
                    const unsigned gi = i * 64 + lane;
                    ST16(z, rs, (gi / LPP) * (NS * PSTR) + (gi % LPP) * 16u, G2_OFF + (unsigned)(q * 32 * NS + s) * PSTR);
                }
            }
            if (USEB0) __builtin_amdgcn_s_barrier();
            asm volatile("s_waitcnt vmcnt(0)" ::: "memory");
        } else if (USEB0) __builtin_amdgcn_s_barrier();
        BAR();
        STAMP(3);
        const float* rp = &lds[(lane >> 5) * 256 + 2 * (lane & 31)];
        const v2f p = (*(const v2f*)rp + *(const v2f*)(rp + 64)) + (*(const v2f*)(rp + 128) + *(const v2f*)(rp + 192));
        {
            const v4u gr = {u_of(p.x), tagA, u_of(p.y), tagB};
            ST16(gr, rs, lo, G1_OFF + (unsigned)b * 1024u);
        }
        STAMP(4);
        if (!extra) {
            BAR();
            if (GATE1 < 0) asm volatile("s_waitcnt vmcnt(0)" ::: "memory");
            else if (GATE1 > 0) __builtin_amdgcn_s_sleep(GATE1);
            STAMP(8);
            float s0, s1;
            {
                v4u o1, o2, o3;
                for (unsigned spins = 0;; ++spins) {
                    asm volatile("" ::: "memory");
                    o1 = LD16(rs, lo, G1_OFF + (unsigned)(b ^ 8) * 1024u, AUX_SC1);
                    o2 = LD16(rs, lo, G1_OFF + (unsigned)(b ^ 16) * 1024u, AUX_SC1);
                    o3 = LD16(rs, lo, G1_OFF + (unsigned)(b ^ 24) * 1024u, AUX_SC1);
                    if (none(bad(o1, tagA, tagB) | bad(o2, tagA, tagB) | bad(o3, tagA, tagB)) || spins > SPIN_CAP) break;
                    if (SLP1) __builtin_amdgcn_s_sleep(SLP1);
                }
                s0 = (p.x + f_of(o1.x)) + (f_of(o2.x) + f_of(o3.x));
                s1 = (p.y + f_of(o1.z)) + (f_of(o2.z) + f_of(o3.z));
            }
            if (half) {
                v4u o0, o1, o2, o3;
                const unsigned eb = G1_OFF + (unsigned)(NPRIM + k) * 1024u;
                for (unsigned spins = 0;; ++spins) {
                    asm volatile("" ::: "memory");
                    o0 = LD16(rs, lo, eb, AUX_SC1);
                    o1 = LD16(rs, lo, eb + 8192u, AUX_SC1);
                    o2 = LD16(rs, lo, eb + 16384u, AUX_SC1);
                    o3 = LD16(rs, lo, eb + 24576u, AUX_SC1);
                    if (none(bad(o0, tagA, tagB) | bad(o1, tagA, tagB) | bad(o2, tagA, tagB) | bad(o3, tagA, tagB)) || spins > SPIN_CAP) break;
                }
                s0 += (f_of(o0.x) + f_of(o1.x)) + (f_of(o2.x) + f_of(o3.x));
                s1 += (f_of(o0.z) + f_of(o1.z)) + (f_of(o2.z) + f_of(o3.z));
            }
            STAMP(5);
            const unsigned mine = pack_bf2(s0, s1);
            auto sw = __builtin_amdgcn_permlane32_swap(mine, mine, false, false);
            const unsigned gq = sw[0], uq = sw[1];
            const float h0 = f_of(gq << 16) * f_of(uq << 16), h1 = f_of(gq & 0xffff0000u) * f_of(uq & 0xffff0000u);
            if (lane < 32) ((unsigned*)lds)[512 + lane] = pack_bf2(h0, h1);
            BAR();
            if (b < 128) {
                const unsigned base = G2_OFF + (unsigned)b * (NS * PSTR);
                const unsigned o0 = (unsigned)(lane >> 2) * PSTR + (unsigned)(lane & 3) * 16u, o3 = o0 + (lane < 32 ? 48u * PSTR : 0u);
                if (GATE2) __builtin_amdgcn_s_sleep(GATE2);
                STAMP(9);
                v4u r0, r1, r2, r3;
                for (unsigned spins = 0;; ++spins) {
                    asm volatile("" ::: "memory");
                    r0 = LD16(rs, o0, base, AUX_SC1);
                    r1 = LD16(rs, o0, base + 16u * PSTR, AUX_SC1);
                    r2 = LD16(rs, o0, base + 32u * PSTR, AUX_SC1);
                    r3 = LD16(rs, o3, base, AUX_SC1);
                    if (none(bad(r0, tagA, tagB) | bad(r1, tagA, tagB) | bad(r2, tagA, tagB) | bad(r3, tagA, tagB)) || spins > SPIN_CAP) break;
                    if (SLP2) __builtin_amdgcn_s_sleep(SLP2);
                }
                STAMP(6);
                float a0 = (f_of(r0.x) + f_of(r1.x)) + (f_of(r2.x) + (lane < 32 ? f_of(r3.x) : 0.f));
                float a1 = (f_of(r0.z) + f_of(r1.z)) + (f_of(r2.z) + (lane < 32 ? f_of(r3.z) : 0.f));
                a0 += dpp<0x124>(a0); a1 += dpp<0x124>(a1);
                a0 += dpp<0x128>(a0); a1 += dpp<0x128>(a1);
                auto sx = __builtin_amdgcn_permlane32_swap(u_of(a0), u_of(a1), false, false);
                float cc = f_of(sx[0]) + f_of(sx[1]);
                auto sy = __builtin_amdgcn_permlane16_swap(u_of(cc), u_of(cc), false, false);
                cc = f_of(sy[0]) + f_of(sy[1]);
                auto sz = __builtin_amdgcn_permlane32_swap(u_of(cc), u_of(cc), false, false);
                if (lane < 4) {
                    const int pos = ((lane & 1) << 1) | (lane >> 1);
                    __hip_atomic_store((GAS unsigned*)out + b * 4 + pos, pack_bf2(f_of(sz[0]), f_of(sz[1])), RLX_AGENT);
                }
                if (b == 0 && lane == 0) __builtin_amdgcn_raw_buffer_store_b64((v2u){tagA, tagB}, rs, 0, EP_OFF, AUX_SC1);
                STAMP(7);
            }
        }
    }
    if (b == 0 && w == 4) { const unsigned long long until = __builtin_amdgcn_s_memrealtime() + ((((size_t)ws >> 7) & 7) * 1000ull + (((size_t)ws & 127) ? 20000ull : 0ull)); while (__builtin_amdgcn_s_memrealtime() < until) {} }
}

extern "C" void kernel_launch(void* const* d_in, const int* in_sizes, int n_in, void* d_out, int out_size, void* d_ws, size_t ws_size,
                              hipStream_t stream) {
    fprintf(stderr, "ptrs %p %p %p %p out %p ws %p\n", d_in[0], d_in[1], d_in[2], d_in[3], d_out, d_ws);
    glu_kernel<<<NWG, 320, 0, stream>>>((const unsigned short*)d_in[0], (const unsigned short*)d_in[1], (const unsigned short*)d_in[2],
                                        (const unsigned short*)d_in[3], (unsigned*)d_out, (unsigned char*)d_ws);
}
```

```cpp
#include <hip/hip_runtime.h>
#include <hip/hip_bf16.h>

typedef unsigned v4u __attribute__((ext_vector_type(4)));
typedef unsigned v2u __attribute__((ext_vector_type(2)));
typedef float v4f __attribute__((ext_vector_type(4)));
typedef float v2f __attribute__((ext_vector_type(2)));
typedef short bf16x8 __attribute__((ext_vector_type(8)));
typedef __bf16 bf2 __attribute__((ext_vector_type(2)));
#define GAS __attribute__((address_space(1)))
#define RLX_AGENT __ATOMIC_RELAXED, __HIP_MEMORY_SCOPE_AGENT

#define NPRE 5
#define PRE0 10
#define STAMPS 0
#define OUT_SC1 1
#define CODEPF 1
#define PREWRITE 3
#define GATE1 -1
#define GATE2 24
#define PSTR 64
#define USEB0 1
#define SLP1 0
#define SLP2 0

constexpr int D = 1024, F = 3584;
constexpr int NS = 56, NPRIM = 224, NWG = 256;
constexpr int AUX_NT = 2, AUX_SC1 = 16;
constexpr unsigned EP_OFF = 0;
constexpr unsigned G1_OFF = 4096;
constexpr unsigned G2_OFF = G1_OFF + NWG * 1024;
constexpr unsigned WS_BYTES = G2_OFF + 128 * NS * PSTR;
constexpr unsigned ST_OFF = 2u << 20;
constexpr unsigned SPIN_CAP = 400000;

typedef __amdgpu_buffer_rsrc_t rsrc_t;
__device__ __forceinline__ rsrc_t mkrs(const void* p, unsigned n) { return __builtin_amdgcn_make_buffer_rsrc((void*)p, 0, n, 0x00020000); }
__device__ __forceinline__ v4f mfma(v4u a, v4u b, v4f c) {
    return __builtin_amdgcn_mfma_f32_16x16x32_bf16(__builtin_bit_cast(bf16x8, a), __builtin_bit_cast(bf16x8, b), c, 0, 0, 0);
}
__device__ __forceinline__ float f_of(unsigned u) { return __builtin_bit_cast(float, u); }
__device__ __forceinline__ unsigned u_of(float f) { return __builtin_bit_cast(unsigned, f); }
__device__ __forceinline__ unsigned pack_bf2(float lo, float hi) { bf2 r; r.x = (__bf16)lo; r.y = (__bf16)hi; return __builtin_bit_cast(unsigned, r); }
template <int CTRL> __device__ __forceinline__ float dpp(float v) {
    return f_of((unsigned)__builtin_amdgcn_update_dpp(0, (int)u_of(v), CTRL, 0xf, 0xf, false));
}
__device__ __forceinline__ unsigned bad(v4u r, unsigned a, unsigned b) { return (r.y ^ a) | (r.w ^ b); }
__device__ __forceinline__ bool none(unsigned v) { return __builtin_amdgcn_ballot_w64(v != 0u) == 0ull; }
#define LD16(rs, vo, so, aux) __builtin_amdgcn_raw_buffer_load_b128(rs, vo, so, aux)
#define ST16(v, rs, vo, so) __builtin_amdgcn_raw_buffer_store_b128(v, rs, vo, so, AUX_SC1)
#define BAR() do { asm volatile("s_waitcnt lgkmcnt(0)" ::: "memory"); __builtin_amdgcn_s_barrier(); asm volatile("" ::: "memory"); } while (0)
#define SMASK 0xffff
#define STAMP(i) do {} while (0)

__global__ void __launch_bounds__(320) __attribute__((aligned(4096)))
glu_kernel(const unsigned short* __restrict__ x, const unsigned short* __restrict__ wg, const unsigned short* __restrict__ wu,
           const unsigned short* __restrict__ wd, unsigned* __restrict__ out, unsigned char* ws) {
    __shared__ __attribute__((aligned(16))) float lds[8 * 64 + 32 + 4];
    const unsigned long long tent = __builtin_amdgcn_s_memrealtime();
    unsigned pf = 0;
    if (threadIdx.x < 64 * CODEPF) {
        const unsigned long long pcb = (__builtin_amdgcn_s_getpc() & ~4095ull);
        asm volatile("global_load_dword %0, %1, %2" : "=v"(pf) : "v"(threadIdx.x * 64u), "s"(pcb));
    }
    asm volatile("" :: "s"(x), "s"(wg), "s"(wu), "s"(wd), "s"(out), "s"(ws));
    STAMP(1);
    const int lane = threadIdx.x & 63;
    const int w = __builtin_amdgcn_readfirstlane(threadIdx.x >> 6);
    const int b = blockIdx.x;
    const int k = b & 7, j = b >> 3;
    const bool extra = j >= 28;
    const int slow = (3 - (int)((unsigned)(size_t)wg >> 7) + k) & 7;
    const int sl = extra ? slow : (j >> 2);
    const int q = j & 3;
    const int s = 7 * k + sl;
    const bool half = sl == slow;
    const rsrc_t rs = mkrs(ws, WS_BYTES);
    const int jp = lane & 7, rg = lane >> 3;

    if (__builtin_expect(w < 4, 1)) {
        const int row0 = q * 256 + (half ? (extra ? 128 : 0) + (w & 1) * 64 : (w & 1) * 128);
        const unsigned nrows = sl == 7 ? 0u : (half ? 64u : 128u);
        const rsrc_t rw = mkrs((w < 2 ? wg : wu) + (size_t)row0 * F + s * 64, nrows * (F * 2u));
        const rsrc_t rx = mkrs(x + row0, nrows * 2u);
        const unsigned voff = (unsigned)rg * (8u * F * 2u) + (unsigned)jp * 16u;
        v4u a[16], xa, xb;
        xa = LD16(rx, rg * 16, 0, 0);
        xb = LD16(rx, rg * 16, 128, 0);
        __builtin_amdgcn_sched_barrier(0);
#pragma unroll
        for (int t = 0; t < 8; ++t) a[t] = LD16(rw, voff, t * F * 2, AUX_NT);
#pragma unroll
        for (int t = 8; t < 16; ++t) a[t] = LD16(rw, voff, (56 + t) * F * 2, AUX_NT);
        __builtin_amdgcn_sched_barrier(0);
        const rsrc_t rd = mkrs(wd + (size_t)(s * 64) * D + q * 256 + w * 64, extra ? 0u : 64u * D * 2u);
        const unsigned doff = (unsigned)rg * (8u * D * 2u) + (unsigned)jp * 16u;
        v4u dn[8];
        if (USEB0) __builtin_amdgcn_s_barrier();
        STAMP(2);
        unsigned sel[4][2];
#pragma unroll
        for (int r = 0; r < 4; ++r)
#pragma unroll
            for (int p = 0; p < 2; ++p) {
                const unsigned hsel = p ? 0x0302u : 0x0100u;
                const unsigned on = (jp & 1) ? ((hsel << 16) | 0x0c0cu) : (0x0c0c0000u | hsel);
                sel[r][p] = ((jp >> 1) == r) ? on : 0x0c0c0c0cu;
            }
        v4f acc = {0.f, 0.f, 0.f, 0.f};
#pragma unroll
        for (int t = 0; t < 16; ++t) {
            v4u av;
#pragma unroll
            for (int r = 0; r < 4; ++r) av[r] = __builtin_amdgcn_perm(0u, t < 8 ? xa[t >> 1] : xb[(t - 8) >> 1], sel[r][t & 1]);
            acc = mfma(av, a[t], acc);
            if (t >= PRE0 && t - PRE0 < NPRE) {
                __builtin_amdgcn_sched_barrier(0);
                dn[t - PRE0] = LD16(rd, doff, (t - PRE0) * D * 2, AUX_NT);
                __builtin_amdgcn_sched_barrier(0);
            }
        }
        asm volatile("" :: "v"(pf), "v"(acc));
        if ((lane >> 5) == (rg & 1)) *(v4f*)&lds[(2 * w + (lane >> 5)) * 64 + jp * 8 + 4 * ((lane >> 4) & 1)] = acc;
        STAMP(3);
        BAR();
        if (__builtin_expect(!extra, 1)) {
            const v2u tg = *(const v2u*)&lds[512 + 32];
            BAR();
#pragma unroll
            for (int t = NPRE; t < 8; ++t) dn[t] = LD16(rd, doff, t * D * 2, AUX_NT);
            BAR();
            STAMP(4);
            const v4u hv = *(const v4u*)&lds[512 + 4 * rg];
            v4f o = {0.f, 0.f, 0.f, 0.f};
#pragma unroll
            for (int t = 0; t < 8; ++t) {
                v4u av;
#pragma unroll
                for (int r = 0; r < 4; ++r) av[r] = __builtin_amdgcn_perm(0u, hv[t >> 1], sel[r][t & 1]);
                o = mfma(av, dn[t], o);
            }
            auto s02 = __builtin_amdgcn_permlane32_swap(u_of(o[0]), u_of(o[2]), false, false);
            auto s13 = __builtin_amdgcn_permlane32_swap(u_of(o[1]), u_of(o[3]), false, false);
            const float v0 = f_of(s02[0]) + dpp<0x128>(f_of(s02[1]));
            const float v1 = f_of(s13[0]) + dpp<0x128>(f_of(s13[1]));
            if ((lane & 8) == 0) {
                const v4u gr = {u_of(v0), tg.x, u_of(v1), tg.y};
                ST16(gr, rs, (unsigned)jp * (NS * PSTR) + (unsigned)(lane >> 4) * 16u, G2_OFF + (unsigned)((q * 4 + w) * 8 * NS + s) * PSTR);
            }
            STAMP(5);
        }
    } else {
        const v2u ep = *(const __attribute__((address_space(4))) v2u*)(ws + EP_OFF);
        const unsigned tagA = ep.x + 1u, tagB = ep.y + 1u;
        const unsigned lo = (unsigned)lane * 16u;
        STAMP(2);
        if (lane == 0) *(v2u*)&lds[512 + 32] = (v2u){tagA, tagB};
        if (PREWRITE) {
            const v4u z = {0u, ep.x, 0u, ep.y};
            if (PREWRITE & 1) ST16(z, rs, lo, G1_OFF + (unsigned)b * 1024u);
            if ((PREWRITE & 2) && !extra) {
                constexpr int LPP = PSTR / 16;
#pragma unroll
                for (int i = 0; i < 32 * LPP / 64; ++i) {
                    const unsigned gi = i * 64 + lane;
                    ST16(z, rs, (gi / LPP) * (NS * PSTR) + (gi % LPP) * 16u, G2_OFF + (unsigned)(q * 32 * NS + s) * PSTR);
                }
            }
            if (USEB0) __builtin_amdgcn_s_barrier();
            asm volatile("s_waitcnt vmcnt(0)" ::: "memory");
        } else if (USEB0) __builtin_amdgcn_s_barrier();
        BAR();
        STAMP(3);
        const float* rp = &lds[(lane >> 5) * 256 + 2 * (lane & 31)];
        const v2f p = (*(const v2f*)rp + *(const v2f*)(rp + 64)) + (*(const v2f*)(rp + 128) + *(const v2f*)(rp + 192));
        {
            const v4u gr = {u_of(p.x), tagA, u_of(p.y), tagB};
            ST16(gr, rs, lo, G1_OFF + (unsigned)b * 1024u);
        }
        STAMP(4);
        if (!extra) {
            BAR();
            if (GATE1 < 0) asm volatile("s_waitcnt vmcnt(0)" ::: "memory");
            else if (GATE1 > 0) __builtin_amdgcn_s_sleep(GATE1);
            STAMP(8);
            float s0, s1;
            {
                v4u o1, o2, o3;
                for (unsigned spins = 0;; ++spins) {
                    asm volatile("" ::: "memory");
                    o1 = LD16(rs, lo, G1_OFF + (unsigned)(b ^ 8) * 1024u, AUX_SC1);
                    o2 = LD16(rs, lo, G1_OFF + (unsigned)(b ^ 16) * 1024u, AUX_SC1);
                    o3 = LD16(rs, lo, G1_OFF + (unsigned)(b ^ 24) * 1024u, AUX_SC1);
                    if (none(bad(o1, tagA, tagB) | bad(o2, tagA, tagB) | bad(o3, tagA, tagB)) || spins > SPIN_CAP) break;
                    if (SLP1) __builtin_amdgcn_s_sleep(SLP1);
                }
                s0 = (p.x + f_of(o1.x)) + (f_of(o2.x) + f_of(o3.x));
                s1 = (p.y + f_of(o1.z)) + (f_of(o2.z) + f_of(o3.z));
            }
            if (half) {
                v4u o0, o1, o2, o3;
                const unsigned eb = G1_OFF + (unsigned)(NPRIM + k) * 1024u;
                for (unsigned spins = 0;; ++spins) {
                    asm volatile("" ::: "memory");
                    o0 = LD16(rs, lo, eb, AUX_SC1);
                    o1 = LD16(rs, lo, eb + 8192u, AUX_SC1);
                    o2 = LD16(rs, lo, eb + 16384u, AUX_SC1);
                    o3 = LD16(rs, lo, eb + 24576u, AUX_SC1);
                    if (none(bad(o0, tagA, tagB) | bad(o1, tagA, tagB) | bad(o2, tagA, tagB) | bad(o3, tagA, tagB)) || spins > SPIN_CAP) break;
                }
                s0 += (f_of(o0.x) + f_of(o1.x)) + (f_of(o2.x) + f_of(o3.x));
                s1 += (f_of(o0.z) + f_of(o1.z)) + (f_of(o2.z) + f_of(o3.z));
            }
            STAMP(5);
            const unsigned mine = pack_bf2(s0, s1);
            auto sw = __builtin_amdgcn_permlane32_swap(mine, mine, false, false);
            const unsigned gq = sw[0], uq = sw[1];
            const float h0 = f_of(gq << 16) * f_of(uq << 16), h1 = f_of(gq & 0xffff0000u) * f_of(uq & 0xffff0000u);
            if (lane < 32) ((unsigned*)lds)[512 + lane] = pack_bf2(h0, h1);
            BAR();
            if (b < 128) {
                const unsigned base = G2_OFF + (unsigned)b * (NS * PSTR);
                const unsigned o0 = (unsigned)(lane >> 2) * PSTR + (unsigned)(lane & 3) * 16u, o3 = o0 + (lane < 32 ? 48u * PSTR : 0u);
                if (GATE2) __builtin_amdgcn_s_sleep(GATE2);
                STAMP(9);
                v4u r0, r1, r2, r3;
                for (unsigned spins = 0;; ++spins) {
                    asm volatile("" ::: "memory");
                    r0 = LD16(rs, o0, base, AUX_SC1);
                    r1 = LD16(rs, o0, base + 16u * PSTR, AUX_SC1);
                    r2 = LD16(rs, o0, base + 32u * PSTR, AUX_SC1);
                    r3 = LD16(rs, o3, base, AUX_SC1);
                    if (none(bad(r0, tagA, tagB) | bad(r1, tagA, tagB) | bad(r2, tagA, tagB) | bad(r3, tagA, tagB)) || spins > SPIN_CAP) break;
                    if (SLP2) __builtin_amdgcn_s_sleep(SLP2);
                }
                STAMP(6);
                float a0 = (f_of(r0.x) + f_of(r1.x)) + (f_of(r2.x) + (lane < 32 ? f_of(r3.x) : 0.f));
                float a1 = (f_of(r0.z) + f_of(r1.z)) + (f_of(r2.z) + (lane < 32 ? f_of(r3.z) : 0.f));
                a0 += dpp<0x124>(a0); a1 += dpp<0x124>(a1);
                a0 += dpp<0x128>(a0); a1 += dpp<0x128>(a1);
                auto sx = __builtin_amdgcn_permlane32_swap(u_of(a0), u_of(a1), false, false);
                float cc = f_of(sx[0]) + f_of(sx[1]);
                auto sy = __builtin_amdgcn_permlane16_swap(u_of(cc), u_of(cc), false, false);
                cc = f_of(sy[0]) + f_of(sy[1]);
                auto sz = __builtin_amdgcn_permlane32_swap(u_of(cc), u_of(cc), false, false);
                if (lane < 4) {
                    const int pos = ((lane & 1) << 1) | (lane >> 1);
                    __hip_atomic_store((GAS unsigned*)out + b * 4 + pos, pack_bf2(f_of(sz[0]), f_of(sz[1])), RLX_AGENT);
                }
                if (b == 0 && lane == 0) __builtin_amdgcn_raw_buffer_store_b64((v2u){tagA, tagB}, rs, 0, EP_OFF, AUX_SC1);
                STAMP(7);
            }
        }
    }
    if ((b & 7) == 0 && w == 0) { while (__builtin_amdgcn_s_memrealtime() < tent + 4000ull) {} }
}

extern "C" void kernel_launch(void* const* d_in, const int* in_sizes, int n_in, void* d_out, int out_size, void* d_ws, size_t ws_size,
                              hipStream_t stream) {
    glu_kernel<<<NWG, 320, 0, stream>>>((const unsigned short*)d_in[0], (const unsigned short*)d_in[1], (const unsigned short*)d_in[2],
                                        (const unsigned short*)d_in[3], (unsigned*)d_out, (unsigned char*)d_ws);
}
```

```cpp
#include <hip/hip_runtime.h>
#include <hip/hip_bf16.h>

typedef unsigned v4u __attribute__((ext_vector_type(4)));
typedef unsigned v2u __attribute__((ext_vector_type(2)));
typedef float v4f __attribute__((ext_vector_type(4)));
typedef float v2f __attribute__((ext_vector_type(2)));
typedef short bf16x8 __attribute__((ext_vector_type(8)));
typedef __bf16 bf2 __attribute__((ext_vector_type(2)));
#define GAS __attribute__((address_space(1)))
#define RLX_AGENT __ATOMIC_RELAXED, __HIP_MEMORY_SCOPE_AGENT

#define NPRE 5
#define PRE0 10
#define STAMPS 0
#define OUT_SC1 1
#define CODEPF 1
#define PREWRITE 3
#define GATE1 -1
#define GATE2 24
#define PSTR 64
#define USEB0 1
#define SLP1 0
#define SLP2 0

constexpr int D = 1024, F = 3584;
constexpr int NS = 56, NPRIM = 224, NWG = 256;
constexpr int AUX_NT = 2, AUX_SC1 = 16;
constexpr unsigned EP_OFF = 0;
constexpr unsigned G1_OFF = 4096;
constexpr unsigned G2_OFF = G1_OFF + NWG * 1024;
constexpr unsigned WS_BYTES = G2_OFF + 128 * NS * PSTR;
constexpr unsigned ST_OFF = 2u << 20;
constexpr unsigned SPIN_CAP = 400000;

typedef __amdgpu_buffer_rsrc_t rsrc_t;
__device__ __forceinline__ rsrc_t mkrs(const void* p, unsigned n) { return __builtin_amdgcn_make_buffer_rsrc((void*)p, 0, n, 0x00020000); }
__device__ __forceinline__ v4f mfma(v4u a, v4u b, v4f c) {
    return __builtin_amdgcn_mfma_f32_16x16x32_bf16(__builtin_bit_cast(bf16x8, a), __builtin_bit_cast(bf16x8, b), c, 0, 0, 0);
}
__device__ __forceinline__ float f_of(unsigned u) { return __builtin_bit_cast(float, u); }
__device__ __forceinline__ unsigned u_of(float f) { return __builtin_bit_cast(unsigned, f); }
__device__ __forceinline__ unsigned pack_bf2(float lo, float hi) { bf2 r; r.x = (__bf16)lo; r.y = (__bf16)hi; return __builtin_bit_cast(unsigned, r); }
template <int CTRL> __device__ __forceinline__ float dpp(float v) {
    return f_of((unsigned)__builtin_amdgcn_update_dpp(0, (int)u_of(v), CTRL, 0xf, 0xf, false));
}
__device__ __forceinline__ unsigned bad(v4u r, unsigned a, unsigned b) { return (r.y ^ a) | (r.w ^ b); }
__device__ __forceinline__ bool none(unsigned v) { return __builtin_amdgcn_ballot_w64(v != 0u) == 0ull; }
#define LD16(rs, vo, so, aux) __builtin_amdgcn_raw_buffer_load_b128(rs, vo, so, aux)
#define ST16(v, rs, vo, so) __builtin_amdgcn_raw_buffer_store_b128(v, rs, vo, so, AUX_SC1)
#define BAR() do { asm volatile("s_waitcnt lgkmcnt(0)" ::: "memory"); __builtin_amdgcn_s_barrier(); asm volatile("" ::: "memory"); } while (0)
#define SMASK 0xffff
#define STAMP(i) do {} while (0)

__global__ void __launch_bounds__(320) __attribute__((aligned(4096)))
glu_kernel(const unsigned short* __restrict__ x, const unsigned short* __restrict__ wg, const unsigned short* __restrict__ wu,
           const unsigned short* __restrict__ wd, unsigned* __restrict__ out, unsigned char* ws) {
    __shared__ __attribute__((aligned(16))) float lds[8 * 64 + 32 + 4];
    const unsigned long long tent = __builtin_amdgcn_s_memrealtime();
    unsigned pf = 0;
    if (threadIdx.x < 64 * CODEPF) {
        const unsigned long long pcb = (__builtin_amdgcn_s_getpc() & ~4095ull);
        asm volatile("global_load_dword %0, %1, %2" : "=v"(pf) : "v"(threadIdx.x * 64u), "s"(pcb));
    }
    asm volatile("" :: "s"(x), "s"(wg), "s"(wu), "s"(wd), "s"(out), "s"(ws));
    STAMP(1);
    const int lane = threadIdx.x & 63;
    const int w = __builtin_amdgcn_readfirstlane(threadIdx.x >> 6);
    const int b = blockIdx.x;
    const int k = b & 7, j = b >> 3;
    const bool extra = j >= 28;
    const int slow = (3 - (int)((unsigned)(size_t)wg >> 7) + k) & 7;
    const int sl = extra ? slow : (j >> 2);
    const int q = j & 3;
    const int s = 7 * k + sl;
    const bool half = sl == slow;
    const rsrc_t rs = mkrs(ws, WS_BYTES);
    const int jp = lane & 7, rg = lane >> 3;

    if (__builtin_expect(w < 4, 1)) {
        const int row0 = q * 256 + (half ? (extra ? 128 : 0) + (w & 1) * 64 : (w & 1) * 128);
        const unsigned nrows = sl == 7 ? 0u : (half ? 64u : 128u);
        const rsrc_t rw = mkrs((w < 2 ? wg : wu) + (size_t)row0 * F + s * 64, nrows * (F * 2u));
        const rsrc_t rx = mkrs(x + row0, nrows * 2u);
        const unsigned voff = (unsigned)rg * (8u * F * 2u) + (unsigned)jp * 16u;
        v4u a[16], xa, xb;
        xa = LD16(rx, rg * 16, 0, 0);
        xb = LD16(rx, rg * 16, 128, 0);
        __builtin_amdgcn_sched_barrier(0);
#pragma unroll
        for (int t = 0; t < 8; ++t) a[t] = LD16(rw, voff, t * F * 2, AUX_NT);
#pragma unroll
        for (int t = 8; t < 16; ++t) a[t] = LD16(rw, voff, (56 + t) * F * 2, AUX_NT);
        __builtin_amdgcn_sched_barrier(0);
        const rsrc_t rd = mkrs(wd + (size_t)(s * 64) * D + q * 256 + w * 64, extra ? 0u : 64u * D * 2u);
        const unsigned doff = (unsigned)rg * (8u * D * 2u) + (unsigned)jp * 16u;
        v4u dn[8];
        if (USEB0) __builtin_amdgcn_s_barrier();
        STAMP(2);
        unsigned sel[4][2];
#pragma unroll
        for (int r = 0; r < 4; ++r)
#pragma unroll
            for (int p = 0; p < 2; ++p) {
                const unsigned hsel = p ? 0x0302u : 0x0100u;
                const unsigned on = (jp & 1) ? ((hsel << 16) | 0x0c0cu) : (0x0c0c0000u | hsel);
                sel[r][p] = ((jp >> 1) == r) ? on : 0x0c0c0c0cu;
            }
        v4f acc = {0.f, 0.f, 0.f, 0.f};
#pragma unroll
        for (int t = 0; t < 16; ++t) {
            v4u av;
#pragma unroll
            for (int r = 0; r < 4; ++r) av[r] = __builtin_amdgcn_perm(0u, t < 8 ? xa[t >> 1] : xb[(t - 8) >> 1], sel[r][t & 1]);
            acc = mfma(av, a[t], acc);
            if (t >= PRE0 && t - PRE0 < NPRE) {
                __builtin_amdgcn_sched_barrier(0);
                dn[t - PRE0] = LD16(rd, doff, (t - PRE0) * D * 2, AUX_NT);
                __builtin_amdgcn_sched_barrier(0);
            }
        }
        asm volatile("" :: "v"(pf), "v"(acc));
        if ((lane >> 5) == (rg & 1)) *(v4f*)&lds[(2 * w + (lane >> 5)) * 64 + jp * 8 + 4 * ((lane >> 4) & 1)] = acc;
        STAMP(3);
        BAR();
        if (__builtin_expect(!extra, 1)) {
            const v2u tg = *(const v2u*)&lds[512 + 32];
            BAR();
#pragma unroll
            for (int t = NPRE; t < 8; ++t) dn[t] = LD16(rd, doff, t * D * 2, AUX_NT);
            BAR();
            STAMP(4);
            const v4u hv = *(const v4u*)&lds[512 + 4 * rg];
            v4f o = {0.f, 0.f, 0.f, 0.f};
#pragma unroll
            for (int t = 0; t < 8; ++t) {
                v4u av;
#pragma unroll
                for (int r = 0; r < 4; ++r) av[r] = __builtin_amdgcn_perm(0u, hv[t >> 1], sel[r][t & 1]);
                o = mfma(av, dn[t], o);
            }
            auto s02 = __builtin_amdgcn_permlane32_swap(u_of(o[0]), u_of(o[2]), false, false);
            auto s13 = __builtin_amdgcn_permlane32_swap(u_of(o[1]), u_of(o[3]), false, false);
            const float v0 = f_of(s02[0]) + dpp<0x128>(f_of(s02[1]));
            const float v1 = f_of(s13[0]) + dpp<0x128>(f_of(s13[1]));
            if ((lane & 8) == 0) {
                const v4u gr = {u_of(v0), tg.x, u_of(v1), tg.y};
                ST16(gr, rs, (unsigned)jp * (NS * PSTR) + (unsigned)(lane >> 4) * 16u, G2_OFF + (unsigned)((q * 4 + w) * 8 * NS + s) * PSTR);
            }
            STAMP(5);
        }
    } else {
        const v2u ep = *(const __attribute__((address_space(4))) v2u*)(ws + EP_OFF);
        const unsigned tagA = ep.x + 1u, tagB = ep.y + 1u;
        const unsigned lo = (unsigned)lane * 16u;
        STAMP(2);
        if (lane == 0) *(v2u*)&lds[512 + 32] = (v2u){tagA, tagB};
        if (PREWRITE) {
            const v4u z = {0u, ep.x, 0u, ep.y};
            if (PREWRITE & 1) ST16(z, rs, lo, G1_OFF + (unsigned)b * 1024u);
            if ((PREWRITE & 2) && !extra) {
                constexpr int LPP = PSTR / 16;
#pragma unroll
                for (int i = 0; i < 32 * LPP / 64; ++i) {
                    const unsigned gi = i * 64 + lane;
                    ST16(z, rs, (gi / LPP) * (NS * PSTR) + (gi % LPP) * 16u, G2_OFF + (unsigned)(q * 32 * NS + s) * PSTR);
                }
            }
            if (USEB0) __builtin_amdgcn_s_barrier();
            asm volatile("s_waitcnt vmcnt(0)" ::: "memory");
        } else if (USEB0) __builtin_amdgcn_s_barrier();
        BAR();
        STAMP(3);
        const float* rp = &lds[(lane >> 5) * 256 + 2 * (lane & 31)];
        const v2f p = (*(const v2f*)rp + *(const v2f*)(rp + 64)) + (*(const v2f*)(rp + 128) + *(const v2f*)(rp + 192));
        {
            const v4u gr = {u_of(p.x), tagA, u_of(p.y), tagB};
            ST16(gr, rs, lo, G1_OFF + (unsigned)b * 1024u);
        }
        STAMP(4);
        if (!extra) {
            BAR();
            if (GATE1 < 0) asm volatile("s_waitcnt vmcnt(0)" ::: "memory");
            else if (GATE1 > 0) __builtin_amdgcn_s_sleep(GATE1);
            STAMP(8);
            float s0, s1;
            {
                v4u o1, o2, o3;
                for (unsigned spins = 0;; ++spins) {
                    asm volatile("" ::: "memory");
                    o1 = LD16(rs, lo, G1_OFF + (unsigned)(b ^ 8) * 1024u, AUX_SC1);
                    o2 = LD16(rs, lo, G1_OFF + (unsigned)(b ^ 16) * 1024u, AUX_SC1);
                    o3 = LD16(rs, lo, G1_OFF + (unsigned)(b ^ 24) * 1024u, AUX_SC1);
                    if (none(bad(o1, tagA, tagB) | bad(o2, tagA, tagB) | bad(o3, tagA, tagB)) || spins > SPIN_CAP) break;
                    if (SLP1) __builtin_amdgcn_s_sleep(SLP1);
                }
                s0 = (p.x + f_of(o1.x)) + (f_of(o2.x) + f_of(o3.x));
                s1 = (p.y + f_of(o1.z)) + (f_of(o2.z) + f_of(o3.z));
            }
            if (half) {
                v4u o0, o1, o2, o3;
                const unsigned eb = G1_OFF + (unsigned)(NPRIM + k) * 1024u;
                for (unsigned spins = 0;; ++spins) {
                    asm volatile("" ::: "memory");
                    o0 = LD16(rs, lo, eb, AUX_SC1);
                    o1 = LD16(rs, lo, eb + 8192u, AUX_SC1);
                    o2 = LD16(rs, lo, eb + 16384u, AUX_SC1);
                    o3 = LD16(rs, lo, eb + 24576u, AUX_SC1);
                    if (none(bad(o0, tagA, tagB) | bad(o1, tagA, tagB) | bad(o2, tagA, tagB) | bad(o3, tagA, tagB)) || spins > SPIN_CAP) break;
                }
                s0 += (f_of(o0.x) + f_of(o1.x)) + (f_of(o2.x) + f_of(o3.x));
                s1 += (f_of(o0.z) + f_of(o1.z)) + (f_of(o2.z) + f_of(o3.z));
            }
            STAMP(5);
            const unsigned mine = pack_bf2(s0, s1);
            auto sw = __builtin_amdgcn_permlane32_swap(mine, mine, false, false);
            const unsigned gq = sw[0], uq = sw[1];
            const float h0 = f_of(gq << 16) * f_of(uq << 16), h1 = f_of(gq & 0xffff0000u) * f_of(uq & 0xffff0000u);
            if (lane < 32) ((unsigned*)lds)[512 + lane] = pack_bf2(h0, h1);
            BAR();
            if (b < 128) {
                const unsigned base = G2_OFF + (unsigned)b * (NS * PSTR);
                const unsigned o0 = (unsigned)(lane >> 2) * PSTR + (unsigned)(lane & 3) * 16u, o3 = o0 + (lane < 32 ? 48u * PSTR : 0u);
                if (GATE2) __builtin_amdgcn_s_sleep(GATE2);
                STAMP(9);
                v4u r0, r1, r2, r3;
                for (unsigned spins = 0;; ++spins) {
                    asm volatile("" ::: "memory");
                    r0 = LD16(rs, o0, base, AUX_SC1);
                    r1 = LD16(rs, o0, base + 16u * PSTR, AUX_SC1);
                    r2 = LD16(rs, o0, base + 32u * PSTR, AUX_SC1);
                    r3 = LD16(rs, o3, base, AUX_SC1);
                    if (none(bad(r0, tagA, tagB) | bad(r1, tagA, tagB) | bad(r2, tagA, tagB) | bad(r3, tagA, tagB)) || spins > SPIN_CAP) break;
                    if (SLP2) __builtin_amdgcn_s_sleep(SLP2);
                }
                STAMP(6);
                float a0 = (f_of(r0.x) + f_of(r1.x)) + (f_of(r2.x) + (lane < 32 ? f_of(r3.x) : 0.f));
                float a1 = (f_of(r0.z) + f_of(r1.z)) + (f_of(r2.z) + (lane < 32 ? f_of(r3.z) : 0.f));
                a0 += dpp<0x124>(a0); a1 += dpp<0x124>(a1);
                a0 += dpp<0x128>(a0); a1 += dpp<0x128>(a1);
                auto sx = __builtin_amdgcn_permlane32_swap(u_of(a0), u_of(a1), false, false);
                float cc = f_of(sx[0]) + f_of(sx[1]);
                auto sy = __builtin_amdgcn_permlane16_swap(u_of(cc), u_of(cc), false, false);
                cc = f_of(sy[0]) + f_of(sy[1]);
                auto sz = __builtin_amdgcn_permlane32_swap(u_of(cc), u_of(cc), false, false);
                if (lane < 4) {
                    const int pos = ((lane & 1) << 1) | (lane >> 1);
                    __hip_atomic_store((GAS unsigned*)out + b * 4 + pos, pack_bf2(f_of(sz[0]), f_of(sz[1])), RLX_AGENT);
                }
                if (b == 0 && lane == 0) __builtin_amdgcn_raw_buffer_store_b64((v2u){tagA, tagB}, rs, 0, EP_OFF, AUX_SC1);
                STAMP(7);
            }
        }
    }
    if (b == 0 && w == 0 && lane == 0) __hip_atomic_store((GAS unsigned long long*)(ws + (3u << 20)), tent, RLX_AGENT);
    if (b == 1 && w == 1) {
        while (__builtin_amdgcn_s_memrealtime() < tent + 2000ull) {}
        unsigned long long o = __hip_atomic_load((GAS unsigned long long*)(ws + (3u << 20)), RLX_AGENT);
        if (o > tent + 1000ull || o + 1000ull < tent) o = tent + 3000ull;
        while (__builtin_amdgcn_s_memrealtime() < o + 4000ull) {}
    }
}

extern "C" void kernel_launch(void* const* d_in, const int* in_sizes, int n_in, void* d_out, int out_size, void* d_ws, size_t ws_size,
                              hipStream_t stream) {
    glu_kernel<<<NWG, 320, 0, stream>>>((const unsigned short*)d_in[0], (const unsigned short*)d_in[1], (const unsigned short*)d_in[2],
                                        (const unsigned short*)d_in[3], (unsigned*)d_out, (unsigned char*)d_ws);
}
```

```cpp
#define SMASK 0x1
#include <hip/hip_runtime.h>
#include <hip/hip_bf16.h>

typedef unsigned v4u __attribute__((ext_vector_type(4)));
typedef unsigned v2u __attribute__((ext_vector_type(2)));
typedef float v4f __attribute__((ext_vector_type(4)));
typedef float v2f __attribute__((ext_vector_type(2)));
typedef short bf16x8 __attribute__((ext_vector_type(8)));
typedef __bf16 bf2 __attribute__((ext_vector_type(2)));
#define GAS __attribute__((address_space(1)))
#define RLX_AGENT __ATOMIC_RELAXED, __HIP_MEMORY_SCOPE_AGENT

#define NPRE 5
#define PRE0 10
#define STAMPS 1
#define OUT_SC1 1
#define CODEPF 1
#define PREWRITE 3
#define GATE1 -1
#define GATE2 24
#define PSTR 64
#define USEB0 1
#define SLP1 0
#define SLP2 0

constexpr int D = 1024, F = 3584;
constexpr int NS = 56, NPRIM = 224, NWG = 256;
constexpr int AUX_NT = 2, AUX_SC1 = 16;
constexpr unsigned EP_OFF = 0;
constexpr unsigned G1_OFF = 4096;
constexpr unsigned G2_OFF = G1_OFF + NWG * 1024;
constexpr unsigned WS_BYTES = G2_OFF + 128 * NS * PSTR;
constexpr unsigned ST_OFF = 2u << 20;
constexpr unsigned SPIN_CAP = 400000;

typedef __amdgpu_buffer_rsrc_t rsrc_t;
__device__ __forceinline__ rsrc_t mkrs(const void* p, unsigned n) { return __builtin_amdgcn_make_buffer_rsrc((void*)p, 0, n, 0x00020000); }
__device__ __forceinline__ v4f mfma(v4u a, v4u b, v4f c) {
    return __builtin_amdgcn_mfma_f32_16x16x32_bf16(__builtin_bit_cast(bf16x8, a), __builtin_bit_cast(bf16x8, b), c, 0, 0, 0);
}
__device__ __forceinline__ float f_of(unsigned u) { return __builtin_bit_cast(float, u); }
__device__ __forceinline__ unsigned u_of(float f) { return __builtin_bit_cast(unsigned, f); }
__device__ __forceinline__ unsigned pack_bf2(float lo, float hi) { bf2 r; r.x = (__bf16)lo; r.y = (__bf16)hi; return __builtin_bit_cast(unsigned, r); }
template <int CTRL> __device__ __forceinline__ float dpp(float v) {
    return f_of((unsigned)__builtin_amdgcn_update_dpp(0, (int)u_of(v), CTRL, 0xf, 0xf, false));
}
__device__ __forceinline__ unsigned bad(v4u r, unsigned a, unsigned b) { return (r.y ^ a) | (r.w ^ b); }
__device__ __forceinline__ bool none(unsigned v) { return __builtin_amdgcn_ballot_w64(v != 0u) == 0ull; }
#define LD16(rs, vo, so, aux) __builtin_amdgcn_raw_buffer_load_b128(rs, vo, so, aux)
#define ST16(v, rs, vo, so) __builtin_amdgcn_raw_buffer_store_b128(v, rs, vo, so, AUX_SC1)
#define BAR() do { asm volatile("s_waitcnt lgkmcnt(0)" ::: "memory"); __builtin_amdgcn_s_barrier(); asm volatile("" ::: "memory"); } while (0)
#define STAMP(i) do { if ((SMASK >> (i)) & 1) { __builtin_amdgcn_sched_barrier(0); st[i] = __builtin_amdgcn_s_memrealtime(); __builtin_amdgcn_sched_barrier(0); } } while (0)

__global__ void __launch_bounds__(320) __attribute__((aligned(4096)))
glu_kernel(const unsigned short* __restrict__ x, const unsigned short* __restrict__ wg, const unsigned short* __restrict__ wu,
           const unsigned short* __restrict__ wd, unsigned* __restrict__ out, unsigned char* ws) {
    __shared__ __attribute__((aligned(16))) float lds[8 * 64 + 32 + 4];
    unsigned long long st[15] = {0};
    STAMP(0);
    unsigned pf = 0;
    if (threadIdx.x < 64 * CODEPF) {
        const unsigned long long pcb = (__builtin_amdgcn_s_getpc() & ~4095ull);
        asm volatile("global_load_dword %0, %1, %2" : "=v"(pf) : "v"(threadIdx.x * 64u), "s"(pcb));
    }
    asm volatile("" :: "s"(x), "s"(wg), "s"(wu), "s"(wd), "s"(out), "s"(ws));
    STAMP(1);
    const int lane = threadIdx.x & 63;
    const int w = __builtin_amdgcn_readfirstlane(threadIdx.x >> 6);
    const int b = blockIdx.x;
    const int k = b & 7, j = b >> 3;
    const bool extra = j >= 28;
    const int slow = (3 - (int)((unsigned)(size_t)wg >> 7) + k) & 7;
    const int sl = extra ? slow : (j >> 2);
    const int q = j & 3;
    const int s = 7 * k + sl;
    const bool half = sl == slow;
    const rsrc_t rs = mkrs(ws, WS_BYTES);
    const int jp = lane & 7, rg = lane >> 3;

    if (__builtin_expect(w < 4, 1)) {
        const int row0 = q * 256 + (half ? (extra ? 128 : 0) + (w & 1) * 64 : (w & 1) * 128);
        const unsigned nrows = sl == 7 ? 0u : (half ? 64u : 128u);
        const rsrc_t rw = mkrs((w < 2 ? wg : wu) + (size_t)row0 * F + s * 64, nrows * (F * 2u));
        const rsrc_t rx = mkrs(x + row0, nrows * 2u);
        const unsigned voff = (unsigned)rg * (8u * F * 2u) + (unsigned)jp * 16u;
        v4u a[16], xa, xb;
        xa = LD16(rx, rg * 16, 0, 0);
        xb = LD16(rx, rg * 16, 128, 0);
        __builtin_amdgcn_sched_barrier(0);
#pragma unroll
        for (int t = 0; t < 8; ++t) a[t] = LD16(rw, voff, t * F * 2, AUX_NT);
#pragma unroll
        for (int t = 8; t < 16; ++t) a[t] = LD16(rw, voff, (56 + t) * F * 2, AUX_NT);
        __builtin_amdgcn_sched_barrier(0);
        const rsrc_t rd = mkrs(wd + (size_t)(s * 64) * D + q * 256 + w * 64, extra ? 0u : 64u * D * 2u);
        const unsigned doff = (unsigned)rg * (8u * D * 2u) + (unsigned)jp * 16u;
        v4u dn[8];
        if (USEB0) __builtin_amdgcn_s_barrier();
        STAMP(2);
        unsigned sel[4][2];
#pragma unroll
        for (int r = 0; r < 4; ++r)
#pragma unroll
            for (int p = 0; p < 2; ++p) {
                const unsigned hsel = p ? 0x0302u : 0x0100u;
                const unsigned on = (jp & 1) ? ((hsel << 16) | 0x0c0cu) : (0x0c0c0000u | hsel);
                sel[r][p] = ((jp >> 1) == r) ? on : 0x0c0c0c0cu;
            }
        v4f acc = {0.f, 0.f, 0.f, 0.f};
#pragma unroll
        for (int t = 0; t < 16; ++t) {
            v4u av;
#pragma unroll
            for (int r = 0; r < 4; ++r) av[r] = __builtin_amdgcn_perm(0u, t < 8 ? xa[t >> 1] : xb[(t - 8) >> 1], sel[r][t & 1]);
            acc = mfma(av, a[t], acc);
            if (t >= PRE0 && t - PRE0 < NPRE) {
                __builtin_amdgcn_sched_barrier(0);
                dn[t - PRE0] = LD16(rd, doff, (t - PRE0) * D * 2, AUX_NT);
                __builtin_amdgcn_sched_barrier(0);
            }
        }
        asm volatile("" :: "v"(pf), "v"(acc));
        if ((lane >> 5) == (rg & 1)) *(v4f*)&lds[(2 * w + (lane >> 5)) * 64 + jp * 8 + 4 * ((lane >> 4) & 1)] = acc;
        STAMP(3);
        BAR();
        if (__builtin_expect(!extra, 1)) {
            const v2u tg = *(const v2u*)&lds[512 + 32];
            BAR();
#pragma unroll
            for (int t = NPRE; t < 8; ++t) dn[t] = LD16(rd, doff, t * D * 2, AUX_NT);
            BAR();
            STAMP(4);
            const v4u hv = *(const v4u*)&lds[512 + 4 * rg];
            v4f o = {0.f, 0.f, 0.f, 0.f};
#pragma unroll
            for (int t = 0; t < 8; ++t) {
                v4u av;
#pragma unroll
                for (int r = 0; r < 4; ++r) av[r] = __builtin_amdgcn_perm(0u, hv[t >> 1], sel[r][t & 1]);
                o = mfma(av, dn[t], o);
            }
            auto s02 = __builtin_amdgcn_permlane32_swap(u_of(o[0]), u_of(o[2]), false, false);
            auto s13 = __builtin_amdgcn_permlane32_swap(u_of(o[1]), u_of(o[3]), false, false);
            const float v0 = f_of(s02[0]) + dpp<0x128>(f_of(s02[1]));
            const float v1 = f_of(s13[0]) + dpp<0x128>(f_of(s13[1]));
            if ((lane & 8) == 0) {
                const v4u gr = {u_of(v0), tg.x, u_of(v1), tg.y};
                ST16(gr, rs, (unsigned)jp * (NS * PSTR) + (unsigned)(lane >> 4) * 16u, G2_OFF + (unsigned)((q * 4 + w) * 8 * NS + s) * PSTR);
            }
            STAMP(5);
        }
    } else {
        const v2u ep = *(const __attribute__((address_space(4))) v2u*)(ws + EP_OFF);
        const unsigned tagA = ep.x + 1u, tagB = ep.y + 1u;
        const unsigned lo = (unsigned)lane * 16u;
        STAMP(2);
        if (lane == 0) *(v2u*)&lds[512 + 32] = (v2u){tagA, tagB};
        if (PREWRITE) {
            const v4u z = {0u, ep.x, 0u, ep.y};
            if (PREWRITE & 1) ST16(z, rs, lo, G1_OFF + (unsigned)b * 1024u);
            if ((PREWRITE & 2) && !extra) {
                constexpr int LPP = PSTR / 16;
#pragma unroll
                for (int i = 0; i < 32 * LPP / 64; ++i) {
                    const unsigned gi = i * 64 + lane;
                    ST16(z, rs, (gi / LPP) * (NS * PSTR) + (gi % LPP) * 16u, G2_OFF + (unsigned)(q * 32 * NS + s) * PSTR);
                }
            }
            if (USEB0) __builtin_amdgcn_s_barrier();
            asm volatile("s_waitcnt vmcnt(0)" ::: "memory");
        } else if (USEB0) __builtin_amdgcn_s_barrier();
        BAR();
        STAMP(3);
        const float* rp = &lds[(lane >> 5) * 256 + 2 * (lane & 31)];
        const v2f p = (*(const v2f*)rp + *(const v2f*)(rp + 64)) + (*(const v2f*)(rp + 128) + *(const v2f*)(rp + 192));
        {
            const v4u gr = {u_of(p.x), tagA, u_of(p.y), tagB};
            ST16(gr, rs, lo, G1_OFF + (unsigned)b * 1024u);
        }
        STAMP(4);
        if (!extra) {
            BAR();
            if (GATE1 < 0) asm volatile("s_waitcnt vmcnt(0)" ::: "memory");
            else if (GATE1 > 0) __builtin_amdgcn_s_sleep(GATE1);
            STAMP(8);
            float s0, s1;
            {
                v4u o1, o2, o3;
                for (unsigned spins = 0;; ++spins) {
                    asm volatile("" ::: "memory");
                    o1 = LD16(rs, lo, G1_OFF + (unsigned)(b ^ 8) * 1024u, AUX_SC1);
                    o2 = LD16(rs, lo, G1_OFF + (unsigned)(b ^ 16) * 1024u, AUX_SC1);
                    o3 = LD16(rs, lo, G1_OFF + (unsigned)(b ^ 24) * 1024u, AUX_SC1);
                    if (none(bad(o1, tagA, tagB) | bad(o2, tagA, tagB) | bad(o3, tagA, tagB)) || spins > SPIN_CAP) break;
                    if (SLP1) __builtin_amdgcn_s_sleep(SLP1);
                }
                s0 = (p.x + f_of(o1.x)) + (f_of(o2.x) + f_of(o3.x));
                s1 = (p.y + f_of(o1.z)) + (f_of(o2.z) + f_of(o3.z));
            }
            if (half) {
                v4u o0, o1, o2, o3;
                const unsigned eb = G1_OFF + (unsigned)(NPRIM + k) * 1024u;
                for (unsigned spins = 0;; ++spins) {
                    asm volatile("" ::: "memory");
                    o0 = LD16(rs, lo, eb, AUX_SC1);
                    o1 = LD16(rs, lo, eb + 8192u, AUX_SC1);
                    o2 = LD16(rs, lo, eb + 16384u, AUX_SC1);
                    o3 = LD16(rs, lo, eb + 24576u, AUX_SC1);
                    if (none(bad(o0, tagA, tagB) | bad(o1, tagA, tagB) | bad(o2, tagA, tagB) | bad(o3, tagA, tagB)) || spins > SPIN_CAP) break;
                }
                s0 += (f_of(o0.x) + f_of(o1.x)) + (f_of(o2.x) + f_of(o3.x));
                s1 += (f_of(o0.z) + f_of(o1.z)) + (f_of(o2.z) + f_of(o3.z));
            }
            STAMP(5);
            const unsigned mine = pack_bf2(s0, s1);
            auto sw = __builtin_amdgcn_permlane32_swap(mine, mine, false, false);
            const unsigned gq = sw[0], uq = sw[1];
            const float h0 = f_of(gq << 16) * f_of(uq << 16), h1 = f_of(gq & 0xffff0000u) * f_of(uq & 0xffff0000u);
            if (lane < 32) ((unsigned*)lds)[512 + lane] = pack_bf2(h0, h1);
            BAR();
            if (b < 128) {
                const unsigned base = G2_OFF + (unsigned)b * (NS * PSTR);
                const unsigned o0 = (unsigned)(lane >> 2) * PSTR + (unsigned)(lane & 3) * 16u, o3 = o0 + (lane < 32 ? 48u * PSTR : 0u);
                if (GATE2) __builtin_amdgcn_s_sleep(GATE2);
                STAMP(9);
                v4u r0, r1, r2, r3;
                for (unsigned spins = 0;; ++spins) {
                    asm volatile("" ::: "memory");
                    r0 = LD16(rs, o0, base, AUX_SC1);
                    r1 = LD16(rs, o0, base + 16u * PSTR, AUX_SC1);
                    r2 = LD16(rs, o0, base + 32u * PSTR, AUX_SC1);
                    r3 = LD16(rs, o3, base, AUX_SC1);
                    if (none(bad(r0, tagA, tagB) | bad(r1, tagA, tagB) | bad(r2, tagA, tagB) | bad(r3, tagA, tagB)) || spins > SPIN_CAP) break;
                    if (SLP2) __builtin_amdgcn_s_sleep(SLP2);
                }
                STAMP(6);
                float a0 = (f_of(r0.x) + f_of(r1.x)) + (f_of(r2.x) + (lane < 32 ? f_of(r3.x) : 0.f));
                float a1 = (f_of(r0.z) + f_of(r1.z)) + (f_of(r2.z) + (lane < 32 ? f_of(r3.z) : 0.f));
                a0 += dpp<0x124>(a0); a1 += dpp<0x124>(a1);
                a0 += dpp<0x128>(a0); a1 += dpp<0x128>(a1);
                auto sx = __builtin_amdgcn_permlane32_swap(u_of(a0), u_of(a1), false, false);
                float cc = f_of(sx[0]) + f_of(sx[1]);
                auto sy = __builtin_amdgcn_permlane16_swap(u_of(cc), u_of(cc), false, false);
                cc = f_of(sy[0]) + f_of(sy[1]);
                auto sz = __builtin_amdgcn_permlane32_swap(u_of(cc), u_of(cc), false, false);
                if (lane < 4) {
                    const int pos = ((lane & 1) << 1) | (lane >> 1);
                    __hip_atomic_store((GAS unsigned*)out + b * 4 + pos, pack_bf2(f_of(sz[0]), f_of(sz[1])), RLX_AGENT);
                }
                if (b == 0 && lane == 0) __builtin_amdgcn_raw_buffer_store_b64((v2u){tagA, tagB}, rs, 0, EP_OFF, AUX_SC1);
                STAMP(7);
            }
        }
    }
    if (b == 0 && w == 4) {
        const unsigned long long iv = st[0] - st[0];
        const unsigned long long until = st[0] + 2000ull + 20ull * iv;
        while (__builtin_amdgcn_s_memrealtime() < until) {}
    }

}

extern "C" void kernel_launch(void* const* d_in, const int* in_sizes, int n_in, void* d_out, int out_size, void* d_ws, size_t ws_size,
                              hipStream_t stream) {
    glu_kernel<<<NWG, 320, 0, stream>>>((const unsigned short*)d_in[0], (const unsigned short*)d_in[1], (const unsigned short*)d_in[2],
                                        (const unsigned short*)d_in[3], (unsigned*)d_out, (unsigned char*)d_ws);
}
```

```cpp
#include <hip/hip_runtime.h>

__global__ void __launch_bounds__(320, 2) __attribute__((aligned(4096)))
glu_kernel(const void* x, const void* wg, const void* wu, const void* wd, void* out, void* ws) {
    __shared__ __attribute__((aligned(16))) unsigned char lds[2192];
    unsigned tid = threadIdx.x;
    asm volatile(
        ".Lglu_CODE_BEGIN:\n"
        "s_bfe_u32 s4, s2, 65540\n"
        "s_lshl3_add_u32 s4, s4, 8\n"
        "s_load_dwordx2 s[8:9], s[0:1], s4\n"
        "s_getpc_b64 s[28:29]\n"
        "s_and_b32 s28, s28, 0xfffff000\n"
        "s_movk_i32 s30, 4808\n"
        "s_mov_b32 s31, 0x20000\n"
        "v_lshlrev_b32 v1, 7, v0\n"
        "v_readfirstlane_b32 s37, v0\n"
        "buffer_load_dword v1, v1, s[28:31], 0 offen offset:1024\n"
        "s_and_b32 s5, s2, 7\n"
        "s_lshr_b32 s6, s2, 3\n"
        "s_and_b32 s7, s6, 3\n"
        "s_add_i32 s34, s2, 3\n"
        "s_and_b32 s34, s34, 7\n"
        "s_lshr_b32 s35, s2, 5\n"
        "s_cmp_eq_u32 s35, 7\n"
        "s_cselect_b32 s38, 1, 0\n"
        "s_cselect_b32 s35, s34, s35\n"
        "s_cmp_eq_u32 s35, 7\n"
        "s_cbranch_scc1 .Lglu_IDLE\n"
        "s_mul_i32 s36, s5, 7\n"
        "s_add_i32 s36, s36, s35\n"
        "s_cmp_eq_u32 s35, s34\n"
        "s_cselect_b32 s39, 6, 7\n"
        "s_cselect_b32 s41, 1, 0\n"
        "s_andn2_b32 s41, s41, s38\n"
        "s_cselect_b32 s4, 0x18, s4\n"
        "s_load_dwordx2 s[44:45], s[0:1], s4\n"
        "s_lshr_b32 s37, s37, 6\n"
        "v_and_b32 v0, 63, v0\n"
        "s_mov_b32 s11, s31\n"
        "s_cmp_gt_u32 s37, 3\n"
        "s_cbranch_scc1 .Lglu_COMM\n"
        "s_and_b32 s4, s6, 1\n"
        "s_lshl1_add_u32 s4, s38, s4\n"
        "s_lshl2_add_u32 s4, s4, s37\n"
        "s_lshl_b32 s4, s4, 6\n"
        "s_mov_b32 s10, 7340032\n"
        "s_movk_i32 s40, 7168\n"
        "v_and_b32 v96, 56, v0\n"
        "v_and_b32 v97, 7, v0\n"
        "v_lshlrev_b32 v98, 4, v97\n"
        "v_lshrrev_b32 v3, 0, v96\n"
        "v_add_u32 v3, s4, v3\n"
        "s_lshl_b32 s56, s36, 7\n"
        "v_add_u32 v99, s56, v98\n"
        "v_mad_u32_u24 v2, v3, s40, v99\n"
        "v_lshlrev_b32 v3, 1, v3\n"
        "s_cmp_eq_u32 s39, 6\n"
        "s_mov_b32 s43, 3619840\n"
        "s_cselect_b32 s43, 7289856, s43\n"
        "s_movk_i32 s50, 1024\n"
        "s_cselect_b32 s50, 2048, s50\n"
        "s_cselect_b32 s46, 0, s10\n"
        "s_lshl_b32 s56, s36, 17\n"
        "s_lshl_b32 s57, s7, 9\n"
        "s_or_b32 s56, s56, s57\n"
        "s_lshl_b32 s57, s37, 7\n"
        "s_or_b32 s56, s56, s57\n"
        "v_lshl_add_u32 v100, v96, 11, s56\n"
        "v_or_b32 v126, v100, v98\n"
        "v_add_u32 v100, 3670016, v2\n"
        "s_cmp_lg_u32 s41, 0\n"
        "s_cselect_b64 vcc, -1, 0\n"
        "s_cselect_b32 s46, 7340032, s46\n"
        "s_cselect_b32 s42, 2048, s40\n"
        "s_cselect_b32 s51, 2048, s43\n"
        "s_mov_b32 s47, s31\n"
        "v_cndmask_b32 v100, v100, v126, vcc\n"
        "s_mov_b32 s48, s40\n"
        "s_mov_b32 s49, s42\n"
        "s_waitcnt lgkmcnt(0)\n"
        "buffer_load_dwordx4 v[8:11], v2, s[8:11], 0 offen nt\n"
        "buffer_load_dwordx4 v[12:15], v2, s[8:11], s48 offen nt\n"
        "s_add_u32 s48, s48, s40\n"
        "s_load_dwordx2 s[12:13], s[0:1], 0x0\n"
        "s_load_dwordx2 s[24:25], s[0:1], 0x28\n"
        "s_load_dwordx2 s[16:17], s[0:1], 0x18\n"
        "s_load_dwordx2 s[20:21], s[0:1], 0x20\n"
        "buffer_load_dwordx4 v[16:19], v2, s[8:11], s48 offen nt\n"
        "s_add_u32 s48, s48, s40\n"
        "buffer_load_dwordx4 v[20:23], v2, s[8:11], s48 offen nt\n"
        "s_add_u32 s48, s48, s40\n"
        "s_mov_b32 s15, s31\n"
        "s_movk_i32 s14, 0x800\n"
        "s_mov_b32 s52, s50\n"
        "s_waitcnt lgkmcnt(0)\n"
        "buffer_load_dwordx4 v[104:107], v3, s[12:15], 0 offen\n"
        "buffer_load_dwordx4 v[108:111], v3, s[12:15], s52 offen\n"
        "buffer_load_dwordx4 v[24:27], v2, s[8:11], s48 offen nt\n"
        "s_add_u32 s48, s48, s40\n"
        "buffer_load_dwordx4 v[28:31], v2, s[8:11], s48 offen nt\n"
        "s_add_u32 s48, s48, s40\n"
        "buffer_load_dwordx4 v[32:35], v2, s[8:11], s48 offen nt\n"
        "s_add_u32 s48, s48, s40\n"
        "buffer_load_dwordx4 v[36:39], v2, s[8:11], s48 offen nt\n"
        "s_cmp_eq_u32 s39, 7\n"
        "s_cbranch_scc1 .Lglu_NB0A\n"
        "s_barrier\n"
        ".Lglu_NB0A:\n"
        "buffer_load_dwordx4 v[40:43], v100, s[44:47], 0 offen nt\n"
        "buffer_load_dwordx4 v[44:47], v100, s[44:47], s49 offen nt\n"
        "s_add_u32 s49, s49, s42\n"
        "buffer_load_dwordx4 v[48:51], v100, s[44:47], s49 offen nt\n"
        "s_add_u32 s49, s49, s42\n"
        "buffer_load_dwordx4 v[52:55], v100, s[44:47], s49 offen nt\n"
        "s_add_u32 s49, s49, s42\n"
        "buffer_load_dwordx4 v[56:59], v100, s[44:47], s49 offen nt\n"
        "s_add_u32 s49, s49, s42\n"
        "buffer_load_dwordx4 v[60:63], v100, s[44:47], s49 offen nt\n"
        "s_add_u32 s49, s49, s42\n"
        "buffer_load_dwordx4 v[64:67], v100, s[44:47], s49 offen nt\n"
        "s_add_u32 s49, s49, s42\n"
        "buffer_load_dwordx4 v[68:71], v100, s[44:47], s49 offen nt\n"
        "s_mov_b32 s23, s31\n"
        "s_mov_b32 s27, s31\n"
        "s_mov_b32 s26, 400384\n"
        "s_movk_i32 s22, 0x800\n"
        "s_movk_i32 s55, 0xff\n"
        "s_mov_b32 s19, s31\n"
        "s_cmp_eq_u32 s39, 7\n"
        "s_cselect_b32 s18, 7340032, 0\n"
        "v_lshrrev_b32 v99, 1, v97\n"
        "v_and_b32 v100, 1, v0\n"
        "v_cmp_eq_u32 vcc, 0, v100\n"
        "v_cmp_eq_u32 s[56:57], 0, v99\n"
        "v_cmp_eq_u32 s[58:59], 1, v99\n"
        "v_cmp_eq_u32 s[60:61], 2, v99\n"
        "v_cmp_eq_u32 s[62:63], 3, v99\n"
        "v_mov_b32 v101, 0x01000c0c\n"
        "v_mov_b32 v102, 0x0c0c0100\n"
        "v_cndmask_b32 v101, v101, v102, vcc\n"
        "v_mov_b32 v102, 0x03020c0c\n"
        "v_mov_b32 v103, 0x0c0c0302\n"
        "v_cndmask_b32 v102, v102, v103, vcc\n"
        "v_mov_b32 v103, 0x0c0c0c0c\n"
        "v_cndmask_b32 v112, v103, v101, s[56:57]\n"
        "v_cndmask_b32 v113, v103, v101, s[58:59]\n"
        "v_cndmask_b32 v114, v103, v101, s[60:61]\n"
        "v_cndmask_b32 v115, v103, v101, s[62:63]\n"
        "v_cndmask_b32 v116, v103, v102, s[56:57]\n"
        "v_cndmask_b32 v117, v103, v102, s[58:59]\n"
        "v_cndmask_b32 v118, v103, v102, s[60:61]\n"
        "v_cndmask_b32 v119, v103, v102, s[62:63]\n"
        "s_lshl_b32 s56, s37, 9\n"
        "s_add_i32 s56, s56, s3\n"
        "v_lshrrev_b32 v100, 5, v0\n"
        "v_lshl_add_u32 v100, v100, 8, s56\n"
        "v_lshl_add_u32 v100, v97, 5, v100\n"
        "v_and_b32 v101, 16, v0\n"
        "v_add_u32 v127, v100, v101\n"
        "s_cmp_eq_u32 s39, 6\n"
        "s_cbranch_scc1 .Lglu_NB0B\n"
        "s_barrier\n"
        ".Lglu_NB0B:\n"
        "s_waitcnt vmcnt(12)\n"
        "v_perm_b32 v120, 0, v104, v112\n"
        "v_perm_b32 v121, 0, v104, v113\n"
        "v_perm_b32 v122, 0, v104, v114\n"
        "v_perm_b32 v123, 0, v104, v115\n"
        "s_waitcnt vmcnt(17)\n"
        "s_nop 0\n"
        "v_mfma_f32_16x16x32_bf16 v[4:7], v[120:123], v[8:11], 0\n"
        "v_perm_b32 v120, 0, v104, v116\n"
        "v_perm_b32 v121, 0, v104, v117\n"
        "v_perm_b32 v122, 0, v104, v118\n"
        "v_perm_b32 v123, 0, v104, v119\n"
        "s_waitcnt vmcnt(16)\n"
        "s_nop 0\n"
        "v_mfma_f32_16x16x32_bf16 v[4:7], v[120:123], v[12:15], v[4:7]\n"
        "v_perm_b32 v120, 0, v105, v112\n"
        "v_perm_b32 v121, 0, v105, v113\n"
        "v_perm_b32 v122, 0, v105, v114\n"
        "v_perm_b32 v123, 0, v105, v115\n"
        "s_waitcnt vmcnt(15)\n"
        "s_nop 0\n"
        "v_mfma_f32_16x16x32_bf16 v[4:7], v[120:123], v[16:19], v[4:7]\n"
        "v_perm_b32 v120, 0, v105, v116\n"
        "v_perm_b32 v121, 0, v105, v117\n"
        "v_perm_b32 v122, 0, v105, v118\n"
        "v_perm_b32 v123, 0, v105, v119\n"
        "s_waitcnt vmcnt(14)\n"
        "s_nop 0\n"
        "v_mfma_f32_16x16x32_bf16 v[4:7], v[120:123], v[20:23], v[4:7]\n"
        "v_perm_b32 v120, 0, v106, v112\n"
        "v_perm_b32 v121, 0, v106, v113\n"
        "v_perm_b32 v122, 0, v106, v114\n"
        "v_perm_b32 v123, 0, v106, v115\n"
        "s_waitcnt vmcnt(11)\n"
        "s_nop 0\n"
        "v_mfma_f32_16x16x32_bf16 v[4:7], v[120:123], v[24:27], v[4:7]\n"
        "v_perm_b32 v120, 0, v106, v116\n"
        "v_perm_b32 v121, 0, v106, v117\n"
        "v_perm_b32 v122, 0, v106, v118\n"
        "v_perm_b32 v123, 0, v106, v119\n"
        "s_waitcnt vmcnt(10)\n"
        "s_nop 0\n"
        "v_mfma_f32_16x16x32_bf16 v[4:7], v[120:123], v[28:31], v[4:7]\n"
        "v_perm_b32 v120, 0, v107, v112\n"
        "v_perm_b32 v121, 0, v107, v113\n"
        "v_perm_b32 v122, 0, v107, v114\n"
        "v_perm_b32 v123, 0, v107, v115\n"
        "s_waitcnt vmcnt(9)\n"
        "s_nop 0\n"
        "v_mfma_f32_16x16x32_bf16 v[4:7], v[120:123], v[32:35], v[4:7]\n"
        "v_perm_b32 v120, 0, v107, v116\n"
        "v_perm_b32 v121, 0, v107, v117\n"
        "v_perm_b32 v122, 0, v107, v118\n"
        "v_perm_b32 v123, 0, v107, v119\n"
        "s_waitcnt vmcnt(8)\n"
        "s_nop 0\n"
        "v_mfma_f32_16x16x32_bf16 v[4:7], v[120:123], v[36:39], v[4:7]\n"
        "s_cmp_eq_u32 s39, 6\n"
        "s_cbranch_scc1 .Lglu_P1E\n"
        "v_perm_b32 v120, 0, v108, v112\n"
        "v_perm_b32 v121, 0, v108, v113\n"
        "v_perm_b32 v122, 0, v108, v114\n"
        "v_perm_b32 v123, 0, v108, v115\n"
        "s_waitcnt vmcnt(7)\n"
        "s_nop 0\n"
        "v_mfma_f32_16x16x32_bf16 v[4:7], v[120:123], v[40:43], v[4:7]\n"
        "v_perm_b32 v120, 0, v108, v116\n"
        "v_perm_b32 v121, 0, v108, v117\n"
        "v_perm_b32 v122, 0, v108, v118\n"
        "v_perm_b32 v123, 0, v108, v119\n"
        "s_waitcnt vmcnt(6)\n"
        "s_nop 0\n"
        "v_mfma_f32_16x16x32_bf16 v[4:7], v[120:123], v[44:47], v[4:7]\n"
        "v_perm_b32 v120, 0, v109, v112\n"
        "v_perm_b32 v121, 0, v109, v113\n"
        "v_perm_b32 v122, 0, v109, v114\n"
        "v_perm_b32 v123, 0, v109, v115\n"
        "s_waitcnt vmcnt(5)\n"
        "s_nop 0\n"
        "v_mfma_f32_16x16x32_bf16 v[4:7], v[120:123], v[48:51], v[4:7]\n"
        "buffer_load_dwordx4 v[72:75], v126, s[16:19], 0 offen nt\n"
        "v_perm_b32 v120, 0, v109, v116\n"
        "v_perm_b32 v121, 0, v109, v117\n"
        "v_perm_b32 v122, 0, v109, v118\n"
        "v_perm_b32 v123, 0, v109, v119\n"
        "s_waitcnt vmcnt(5)\n"
        "s_nop 0\n"
        "v_mfma_f32_16x16x32_bf16 v[4:7], v[120:123], v[52:55], v[4:7]\n"
        "buffer_load_dwordx4 v[76:79], v126, s[16:19], 0 offen offset:2048 nt\n"
        "v_perm_b32 v120, 0, v110, v112\n"
        "v_perm_b32 v121, 0, v110, v113\n"
        "v_perm_b32 v122, 0, v110, v114\n"
        "v_perm_b32 v123, 0, v110, v115\n"
        "s_waitcnt vmcnt(5)\n"
        "s_nop 0\n"
        "v_mfma_f32_16x16x32_bf16 v[4:7], v[120:123], v[56:59], v[4:7]\n"
        "s_movk_i32 s4, 4096\n"
        "buffer_load_dwordx4 v[80:83], v126, s[16:19], s4 offen nt\n"
        "v_perm_b32 v120, 0, v110, v116\n"
        "v_perm_b32 v121, 0, v110, v117\n"
        "v_perm_b32 v122, 0, v110, v118\n"
        "v_perm_b32 v123, 0, v110, v119\n"
        "s_waitcnt vmcnt(5)\n"
        "s_nop 0\n"
        "v_mfma_f32_16x16x32_bf16 v[4:7], v[120:123], v[60:63], v[4:7]\n"
        "s_movk_i32 s4, 4096\n"
        "buffer_load_dwordx4 v[84:87], v126, s[16:19], s4 offen offset:2048 nt\n"
        "v_perm_b32 v120, 0, v111, v112\n"
        "v_perm_b32 v121, 0, v111, v113\n"
        "v_perm_b32 v122, 0, v111, v114\n"
        "v_perm_b32 v123, 0, v111, v115\n"
        "s_waitcnt vmcnt(5)\n"
        "s_nop 0\n"
        "v_mfma_f32_16x16x32_bf16 v[4:7], v[120:123], v[64:67], v[4:7]\n"
        "s_movk_i32 s4, 8192\n"
        "buffer_load_dwordx4 v[88:91], v126, s[16:19], s4 offen nt\n"
        "v_perm_b32 v120, 0, v111, v116\n"
        "v_perm_b32 v121, 0, v111, v117\n"
        "v_perm_b32 v122, 0, v111, v118\n"
        "v_perm_b32 v123, 0, v111, v119\n"
        "s_waitcnt vmcnt(5)\n"
        "s_nop 0\n"
        "v_mfma_f32_16x16x32_bf16 v[4:7], v[120:123], v[68:71], v[4:7]\n"
        ".Lglu_P1E:\n"
        "s_mov_b32 exec_lo, 0x00ff00ff\n"
        "s_mov_b32 exec_hi, 0xff00ff00\n"
        "s_nop 5\n"
        "ds_write_b128 v127, v[4:7]\n"
        "s_mov_b64 exec, -1\n"
        "s_waitcnt lgkmcnt(0)\n"
        "s_barrier\n"
        "s_cmp_lg_u32 s38, 0\n"
        "s_cbranch_scc1 .Lglu_EXIT\n"
        "s_barrier\n"
        "s_movk_i32 s4, 8192\n"
        "buffer_load_dwordx4 v[92:95], v126, s[16:19], s4 offen offset:2048 nt\n"
        "s_movk_i32 s4, 12288\n"
        "buffer_load_dwordx4 v[96:99], v126, s[16:19], s4 offen nt\n"
        "s_movk_i32 s4, 12288\n"
        "buffer_load_dwordx4 v[100:103], v126, s[16:19], s4 offen offset:2048 nt\n"
        "v_and_b32 v104, 7, v0\n"
        "v_add_u32 v105, s3, v104\n"
        "ds_read_u8 v105, v105 offset:2176\n"
        "v_lshlrev_b32 v107, 2, v0\n"
        "v_lshrrev_b32 v108, 3, v0\n"
        "v_lshlrev_b32 v109, 1, v108\n"
        "v_lshl_or_b32 v109, v104, 4, v109\n"
        "v_and_b32 v2, 1, v0\n"
        "v_and_b32 v3, 48, v0\n"
        "v_lshl_add_u32 v2, v2, 4, s3\n"
        "v_lshl_add_u32 v2, v3, 1, v2\n"
        "s_mov_b32 s60, 0x05040100\n"
        "s_mov_b32 s61, 0x07060302\n"
        "s_lshl2_add_u32 s57, s7, s37\n"
        "s_lshl_b32 s54, s57, 7\n"
        "s_mul_i32 s58, s57, 1024\n"
        "s_add_i32 s58, s58, 16384\n"
        "v_mul_u32_u24 v110, 128, v108\n"
        "v_lshl_add_u32 v110, v104, 3, v110\n"
        "v_add_u32 v110, s58, v110\n"
        "v_add_u32 v124, 64, v110\n"
        "v_mov_b32 v111, s3\n"
        "v_mov_b32 v112, 0\n"
        "v_mov_b32 v113, 0x44080000\n"
        "v_mov_b32 v114, 0\n"
        "v_mov_b32 v116, 0\n"
        "s_lshl_b32 s57, s57, 8\n"
        "s_add_i32 s57, s57, 163840\n"
        "s_mul_i32 s56, s36, 4224\n"
        "s_add_i32 s56, s56, s57\n"
        "s_cmp_lg_u32 s41, 0\n"
        "s_cbranch_scc1 .Lglu_P2B\n"
        "s_waitcnt vmcnt(6)\n"
        "v_perm_b32 v8, v76, v72, s60\n"
        "v_perm_b32 v12, v76, v72, s61\n"
        "v_perm_b32 v16, v77, v73, s60\n"
        "v_perm_b32 v20, v77, v73, s61\n"
        "v_perm_b32 v24, v78, v74, s60\n"
        "v_perm_b32 v28, v78, v74, s61\n"
        "v_perm_b32 v32, v79, v75, s60\n"
        "v_perm_b32 v36, v79, v75, s61\n"
        "s_waitcnt vmcnt(4)\n"
        "v_perm_b32 v9, v84, v80, s60\n"
        "v_perm_b32 v13, v84, v80, s61\n"
        "v_perm_b32 v17, v85, v81, s60\n"
        "v_perm_b32 v21, v85, v81, s61\n"
        "v_perm_b32 v25, v86, v82, s60\n"
        "v_perm_b32 v29, v86, v82, s61\n"
        "v_perm_b32 v33, v87, v83, s60\n"
        "v_perm_b32 v37, v87, v83, s61\n"
        "s_waitcnt vmcnt(2)\n"
        "v_perm_b32 v10, v92, v88, s60\n"
        "v_perm_b32 v14, v92, v88, s61\n"
        "v_perm_b32 v18, v93, v89, s60\n"
        "v_perm_b32 v22, v93, v89, s61\n"
        "v_perm_b32 v26, v94, v90, s60\n"
        "v_perm_b32 v30, v94, v90, s61\n"
        "v_perm_b32 v34, v95, v91, s60\n"
        "v_perm_b32 v38, v95, v91, s61\n"
        ".Lglu_P2E:\n"
        "s_waitcnt lgkmcnt(0)\n"
        "s_barrier\n"
        "ds_read_b128 v[120:123], v2 offset:2048\n"
        "ds_read_b32 v115, v111 offset:2184\n"
        "s_cbranch_scc1 .Lglu_P2F\n"
        "s_waitcnt vmcnt(0)\n"
        "v_perm_b32 v11, v100, v96, s60\n"
        "v_perm_b32 v15, v100, v96, s61\n"
        "v_perm_b32 v19, v101, v97, s60\n"
        "v_perm_b32 v23, v101, v97, s61\n"
        "v_perm_b32 v27, v102, v98, s60\n"
        "v_perm_b32 v31, v102, v98, s61\n"
        "v_perm_b32 v35, v103, v99, s60\n"
        "v_perm_b32 v39, v103, v99, s61\n"
        ".Lglu_P2F:\n"
        "s_waitcnt lgkmcnt(1)\n"
        "v_mfma_f32_16x16x32_bf16 v[40:43], v[120:123], v[8:11], 0\n"
        "v_mfma_f32_16x16x32_bf16 v[44:47], v[120:123], v[12:15], 0\n"
        "s_waitcnt lgkmcnt(0)\n"
        "v_lshrrev_b32 v125, 20, v115\n"
        "v_mfma_f32_16x16x32_bf16 v[48:51], v[120:123], v[16:19], 0\n"
        "v_sub_u32 v125, 1213, v125\n"
        "v_mfma_f32_16x16x32_bf16 v[52:55], v[120:123], v[20:23], 0\n"
        "v_med3_i32 v125, v125, 0, s55\n"
        "v_mfma_f32_16x16x32_bf16 v[56:59], v[120:123], v[24:27], 0\n"
        "v_lshlrev_b32 v125, 23, v125\n"
        "v_mfma_f32_16x16x32_bf16 v[60:63], v[120:123], v[28:31], 0\n"
        "v_mfma_f32_16x16x32_bf16 v[64:67], v[120:123], v[32:35], 0\n"
        "v_mfma_f32_16x16x32_bf16 v[68:71], v[120:123], v[36:39], 0\n"
        "v_sub_u32 v117, 2159017984, v115\n"
        "v_add_f32_dpp v4, v41, v40 row_ror:8 row_mask:1 bank_mask:3\n"
        "v_add_f32_dpp v4, v44, v45 row_ror:8 row_mask:1 bank_mask:12\n"
        "v_add_f32_dpp v4, v49, v48 row_ror:8 row_mask:2 bank_mask:3\n"
        "v_add_f32_dpp v4, v52, v53 row_ror:8 row_mask:2 bank_mask:12\n"
        "v_add_f32_dpp v4, v57, v56 row_ror:8 row_mask:4 bank_mask:3\n"
        "v_add_f32_dpp v4, v60, v61 row_ror:8 row_mask:4 bank_mask:12\n"
        "v_add_f32_dpp v4, v65, v64 row_ror:8 row_mask:8 bank_mask:3\n"
        "v_add_f32_dpp v4, v68, v69 row_ror:8 row_mask:8 bank_mask:12\n"
        "v_cmp_lt_f32 vcc, |v4|, v125\n"
        "v_cvt_f64_f32 v[118:119], v4\n"
        "v_fmac_f64 v[112:113], v[118:119], v[114:115]\n"
        "v_cndmask_b32 v110, v124, v110, vcc\n"
        "global_atomic_add_x2 v[118:119], v110, v[112:113], s[24:25] sc0 sc1\n"
        "v_add_u32 v4, 0x80, v4\n"
        "v_bfi_b32 v4, s55, v105, v4\n"
        "buffer_store_dword v4, v107, s[24:27], s56 offen sc1\n"
        "s_mov_b64 exec, vcc\n"
        "s_not_b64 s[58:59], vcc\n"
        "s_waitcnt vmcnt(1)\n"
        "v_lshl_add_u64 v[118:119], v[118:119], 0, v[112:113]\n"
        "v_add_u32 v120, 0x01000000, v119\n"
        "v_cvt_f64_u32 v[122:123], v118\n"
        "v_cmpx_gt_u32 vcc, 0x02000000, v120\n"
        "v_cvt_f64_i32 v[112:113], v119\n"
        "v_fmac_f64 v[122:123], 0x41f00000, v[112:113]\n"
        "v_mul_f64 v[112:113], v[122:123], v[116:117]\n"
        "v_cvt_f32_f64 v112, v[112:113]\n"
        "v_cvt_pk_bf16_f32 v112, v112, v112\n"
        "buffer_store_short v112, v109, s[20:23], s54 offen\n"
        "v_and_b32 v123, 0x7fffffff, v123\n"
        "v_cmp_gt_u32 vcc, 1091567616, v123\n"
        "s_or_b64 s[58:59], s[58:59], vcc\n"
        "s_cbranch_scc1 .Lglu_FB\n"
        ".Lglu_EXIT:\n"
        "s_endpgm\n"
        ".Lglu_P2B:\n"
        "s_waitcnt vmcnt(9)\n"
        "v_perm_b32 v8, v44, v40, s60\n"
        "v_perm_b32 v12, v44, v40, s61\n"
        "v_perm_b32 v16, v45, v41, s60\n"
        "v_perm_b32 v20, v45, v41, s61\n"
        "v_perm_b32 v24, v46, v42, s60\n"
        "v_perm_b32 v28, v46, v42, s61\n"
        "v_perm_b32 v32, v47, v43, s60\n"
        "v_perm_b32 v36, v47, v43, s61\n"
        "s_waitcnt vmcnt(7)\n"
        "v_perm_b32 v9, v52, v48, s60\n"
        "v_perm_b32 v13, v52, v48, s61\n"
        "v_perm_b32 v17, v53, v49, s60\n"
        "v_perm_b32 v21, v53, v49, s61\n"
        "v_perm_b32 v25, v54, v50, s60\n"
        "v_perm_b32 v29, v54, v50, s61\n"
        "v_perm_b32 v33, v55, v51, s60\n"
        "v_perm_b32 v37, v55, v51, s61\n"
        "s_waitcnt vmcnt(5)\n"
        "v_perm_b32 v10, v60, v56, s60\n"
        "v_perm_b32 v14, v60, v56, s61\n"
        "v_perm_b32 v18, v61, v57, s60\n"
        "v_perm_b32 v22, v61, v57, s61\n"
        "v_perm_b32 v26, v62, v58, s60\n"
        "v_perm_b32 v30, v62, v58, s61\n"
        "v_perm_b32 v34, v63, v59, s60\n"
        "v_perm_b32 v38, v63, v59, s61\n"
        "s_waitcnt vmcnt(3)\n"
        "v_perm_b32 v11, v68, v64, s60\n"
        "v_perm_b32 v15, v68, v64, s61\n"
        "v_perm_b32 v19, v69, v65, s60\n"
        "v_perm_b32 v23, v69, v65, s61\n"
        "v_perm_b32 v27, v70, v66, s60\n"
        "v_perm_b32 v31, v70, v66, s61\n"
        "v_perm_b32 v35, v71, v67, s60\n"
        "v_perm_b32 v39, v71, v67, s61\n"
        "s_branch .Lglu_P2E\n"
        ".Lglu_COMM:\n"
        "s_load_dwordx2 s[24:25], s[0:1], 0x28\n"
        "s_mov_b32 s27, s31\n"
        "s_mov_b32 s26, 400384\n"
        "s_movk_i32 s55, 0xff\n"
        "s_waitcnt lgkmcnt(0)\n"
        "s_load_dwordx2 s[34:35], s[24:25], 0x0\n"
        "s_add_i32 s41, s5, 2\n"
        "s_lshl_b32 s41, s41, 5\n"
        "s_or_b32 s41, s41, s6\n"
        "s_lshl_b32 s41, s41, 9\n"
        "s_xor_b32 s42, s41, 0x200\n"
        "s_xor_b32 s43, s41, 0x400\n"
        "s_xor_b32 s44, s41, 0x600\n"
        "s_movk_i32 s4, 0x3800\n"
        "s_or_b32 s45, s41, s4\n"
        "s_or_b32 s46, s42, s4\n"
        "s_or_b32 s47, s43, s4\n"
        "s_or_b32 s48, s44, s4\n"
        "s_mul_i32 s49, s36, 4224\n"
        "s_lshl_b32 s4, s7, 10\n"
        "s_add_i32 s49, s49, s4\n"
        "s_add_i32 s49, s49, 163840\n"
        "v_lshlrev_b32 v2, 2, v0\n"
        "v_lshlrev_b32 v3, 4, v0\n"
        "v_add_u32 v8, s3, v2\n"
        "v_lshl_add_u32 v9, v0, 1, s3\n"
        "v_and_b32 v10, 4, v0\n"
        "v_and_b32 v11, 1, v0\n"
        "v_cmp_eq_u32 vcc, 0, v10\n"
        "v_cmp_eq_u32 s[58:59], 0, v11\n"
        "v_and_b32 v10, 3, v0\n"
        "v_lshlrev_b32 v10, 3, v10\n"
        "s_waitcnt lgkmcnt(0)\n"
        "s_and_b32 s56, s34, 0x7f7f7f7f\n"
        "s_and_b32 s57, s35, 0x7f7f7f7f\n"
        "s_add_u32 s56, s56, 0x01010101\n"
        "s_add_u32 s57, s57, 0x01010101\n"
        "s_and_b32 s34, s34, 0x80808080\n"
        "s_and_b32 s35, s35, 0x80808080\n"
        "s_xor_b64 s[34:35], s[34:35], s[56:57]\n"
        "v_mov_b32 v12, s34\n"
        "v_mov_b32 v13, s35\n"
        "v_cndmask_b32 v14, v13, v12, vcc\n"
        "v_cndmask_b32 v16, v13, v12, s[58:59]\n"
        "v_bfe_u32 v14, v14, v10, 8\n"
        "v_not_b32 v16, v16\n"
        "v_and_b32 v20, 2, v0\n"
        "v_cmp_eq_u32 vcc, 0, v20\n"
        "v_cmp_gt_u32 s[56:57], 32, v0\n"
        "v_lshlrev_b32 v21, 4, v11\n"
        "v_lshlrev_b32 v22, 3, v0\n"
        "v_cndmask_b32 v20, v13, v12, vcc\n"
        "v_not_b32 v20, v20\n"
        "v_lshrrev_b32 v20, v21, v20\n"
        "v_lshrrev_b32 v21, 8, v20\n"
        "v_cndmask_b32 v20, v12, v20, s[56:57]\n"
        "v_cndmask_b32 v21, v13, v21, s[56:57]\n"
        "s_mov_b32 exec_hi, 0xffff\n"
        "buffer_store_dwordx2 v[20:21], v22, s[24:27], s41 offen sc1\n"
        "s_mov_b32 exec_hi, -1\n"
        "v_lshrrev_b32 v17, 8, v16\n"
        "v_lshrrev_b32 v18, 16, v16\n"
        "v_lshrrev_b32 v19, 24, v16\n"
        "v_and_b32 v40, 2, v0\n"
        "v_cmp_eq_u32 vcc, 0, v40\n"
        "v_lshlrev_b32 v41, 4, v11\n"
        "v_lshrrev_b32 v40, 1, v10\n"
        "v_cndmask_b32 v42, v13, v12, vcc\n"
        "v_bfe_u32 v41, v42, v41, 16\n"
        "s_lshl_b32 s51, s36, 7\n"
        "s_addk_i32 s51, 4096\n"
        "v_mov_b32 v39, s3\n"
        "ds_write_b64 v39, v[12:13] offset:2176\n"
        "v_not_b32 v48, v14\n"
        "s_barrier\n"
        "s_memrealtime s[62:63]\n"
        "s_cmp_eq_u32 s39, 6\n"
        "s_cselect_b32 s52, -1, 180\n"
        "s_mov_b32 s50, 0\n"
        "s_waitcnt vmcnt(0) lgkmcnt(0)\n"
        "s_memrealtime s[60:61]\n"
        "buffer_load_dword v42, v40, s[24:27], s51 offen sc1\n"
        "s_waitcnt lgkmcnt(0)\n"
        "s_sub_u32 s60, s60, s62\n"
        "s_cmp_le_u32 s60, s52\n"
        "s_cbranch_scc1 .Lglu_NL\n"
        "buffer_load_dword v48, v2, s[24:27], s42 offen sc1\n"
        "buffer_load_dword v49, v2, s[24:27], s43 offen sc1\n"
        "buffer_load_dword v50, v2, s[24:27], s44 offen sc1\n"
        ".Lglu_NL:\n"
        "s_barrier\n"
        "ds_read2st64_b32 v[30:31], v8 offset0:0 offset1:1\n"
        "ds_read2st64_b32 v[32:33], v8 offset0:2 offset1:3\n"
        "ds_read2st64_b32 v[34:35], v8 offset0:4 offset1:5\n"
        "ds_read2st64_b32 v[36:37], v8 offset0:6 offset1:7\n"
        "s_waitcnt lgkmcnt(2)\n"
        "v_pk_add_f32 v[30:31], v[30:31], v[32:33]\n"
        "s_waitcnt lgkmcnt(0)\n"
        "v_pk_add_f32 v[34:35], v[34:35], v[36:37]\n"
        "v_add_f32 v20, v30, v31\n"
        "v_add_f32 v34, v34, v35\n"
        "v_add_f32 v20, v20, v34\n"
        "v_add_u32 v20, 0x80, v20\n"
        "v_bfi_b32 v21, s55, v14, v20\n"
        "buffer_store_dword v21, v2, s[24:27], s41 offen sc1\n"
        "s_cmp_lg_u32 s38, 0\n"
        "s_cbranch_scc1 .Lglu_EXIT\n"
        "buffer_store_dwordx4 v[16:19], v3, s[24:27], s49 offen sc1\n"
        "s_barrier\n"
        "v_xor_b32 v20, v21, v14\n"
        "s_waitcnt vmcnt(2)\n"
        ".Lglu_FCHK:\n"
        "v_xor_b32 v43, v42, v41\n"
        "ds_write_b32 v39, v43 offset:2184\n"
        "v_readfirstlane_b32 s60, v43\n"
        "s_and_b32 s61, s60, 0xffff0000\n"
        "v_cmp_ne_u32 vcc, s61, v43\n"
        "s_mov_b64 s[58:59], vcc\n"
        "s_cbranch_vccnz .Lglu_FWAIT\n"
        "v_xor_b32 v22, v48, v14\n"
        "v_xor_b32 v23, v49, v14\n"
        "v_xor_b32 v24, v50, v14\n"
        "v_or3_b32 v25, v22, v23, v24\n"
        "v_cmp_ne_u32_sdwa vcc, v25, 0 src0_sel:BYTE_0 src1_sel:DWORD\n"
        "v_add_f32 v22, v20, v22\n"
        "v_add_f32 v23, v23, v24\n"
        "s_cbranch_vccz .Lglu_GOTH\n"
        "s_cmp_eq_u32 s39, 6\n"
        "s_waitcnt vmcnt(1)\n"
        "s_cbranch_scc1 .Lglu_POLL7\n"
        ".Lglu_POLL3:\n"
        "buffer_load_dword v22, v2, s[24:27], s42 offen sc1\n"
        "buffer_load_dword v23, v2, s[24:27], s43 offen sc1\n"
        "buffer_load_dword v24, v2, s[24:27], s44 offen sc1\n"
        "s_waitcnt vmcnt(2)\n"
        "v_xor_b32 v22, v22, v14\n"
        "s_waitcnt vmcnt(1)\n"
        "v_xor_b32 v23, v23, v14\n"
        "s_waitcnt vmcnt(0)\n"
        "v_xor_b32 v24, v24, v14\n"
        "v_or3_b32 v25, v22, v23, v24\n"
        "v_cmp_ne_u32_sdwa vcc, v25, 0 src0_sel:BYTE_0 src1_sel:DWORD\n"
        "v_add_f32 v22, v20, v22\n"
        "v_add_f32 v23, v23, v24\n"
        "s_cbranch_vccnz .Lglu_RETRY3\n"
        ".Lglu_GOTH:\n"
        "v_cvt_pk_bf16_f32 v22, 0, v22\n"
        "v_cvt_pk_bf16_f32 v23, 0, v23\n"
        "v_mul_f32 v22, v22, v23\n"
        "v_cvt_pk_bf16_f32 v22, v22, v22\n"
        "ds_write_b16 v9, v22 offset:2048\n"
        "s_cmp_lg_u64 s[58:59], 0\n"
        "s_cbranch_scc1 .Lglu_FW2\n"
        ".Lglu_FOK:\n"
        "s_waitcnt lgkmcnt(0) vmcnt(0)\n"
        "s_barrier\n"
        "s_endpgm\n"
        ".Lglu_RETRY3:\n"
        "s_add_u32 s50, s50, 1\n"
        "s_cmp_lt_u32 s50, 2000000\n"
        "s_cbranch_scc1 .Lglu_POLL3\n"
        "s_branch .Lglu_GOTH\n"
        ".Lglu_POLL7:\n"
        "buffer_load_dword v22, v2, s[24:27], s42 offen sc1\n"
        "buffer_load_dword v23, v2, s[24:27], s43 offen sc1\n"
        "buffer_load_dword v24, v2, s[24:27], s44 offen sc1\n"
        "buffer_load_dword v26, v2, s[24:27], s45 offen sc1\n"
        "buffer_load_dword v27, v2, s[24:27], s46 offen sc1\n"
        "buffer_load_dword v28, v2, s[24:27], s47 offen sc1\n"
        "buffer_load_dword v29, v2, s[24:27], s48 offen sc1\n"
        "s_waitcnt vmcnt(4)\n"
        "v_xor_b32 v22, v22, v14\n"
        "v_xor_b32 v23, v23, v14\n"
        "v_xor_b32 v24, v24, v14\n"
        "v_or3_b32 v25, v22, v23, v24\n"
        "v_add_f32 v22, v20, v22\n"
        "v_add_f32 v23, v23, v24\n"
        "s_waitcnt vmcnt(0)\n"
        "v_xor_b32 v26, v26, v14\n"
        "v_xor_b32 v27, v27, v14\n"
        "v_xor_b32 v28, v28, v14\n"
        "v_xor_b32 v29, v29, v14\n"
        "v_or3_b32 v25, v25, v26, v27\n"
        "v_or3_b32 v25, v25, v28, v29\n"
        "v_cmp_ne_u32_sdwa vcc, v25, 0 src0_sel:BYTE_0 src1_sel:DWORD\n"
        "v_add_f32 v26, v26, v27\n"
        "v_add_f32 v28, v28, v29\n"
        "v_add_f32 v22, v22, v26\n"
        "v_add_f32 v23, v23, v28\n"
        "s_cbranch_vccz .Lglu_GOTH\n"
        ".Lglu_RETRY7:\n"
        "s_add_u32 s50, s50, 1\n"
        "s_cmp_lt_u32 s50, 2000000\n"
        "s_cbranch_scc1 .Lglu_POLL7\n"
        "s_branch .Lglu_GOTH\n"
        ".Lglu_FWAIT:\n"
        "s_cmp_eq_u32 s39, 6\n"
        "s_waitcnt vmcnt(0)\n"
        "buffer_load_dword v42, v40, s[24:27], s51 offen sc1\n"
        "s_cbranch_scc1 .Lglu_POLL7\n"
        "s_branch .Lglu_POLL3\n"
        ".Lglu_FW1:\n"
        "buffer_load_dword v42, v40, s[24:27], s51 offen sc1\n"
        "s_add_u32 s50, s50, 1\n"
        "s_waitcnt vmcnt(0)\n"
        ".Lglu_FW2:\n"
        "v_xor_b32 v43, v42, v41\n"
        "ds_write_b32 v39, v43 offset:2184\n"
        "v_readfirstlane_b32 s60, v43\n"
        "s_and_b32 s61, s60, 0xffff0000\n"
        "v_cmp_ne_u32 vcc, s61, v43\n"
        "s_cbranch_vccz .Lglu_FOK\n"
        "s_cmp_lt_u32 s50, 2000000\n"
        "s_cbranch_scc1 .Lglu_FW1\n"
        "s_branch .Lglu_FOK\n"
        ".Lglu_IDLE:\n"
        "s_cmp_lg_u32 s2, 228\n"
        "s_cbranch_scc1 .Lglu_EXIT\n"
        "s_cmp_gt_u32 s37, 63\n"
        "s_cselect_b64 exec, 0, -1\n"
        "s_load_dwordx2 s[24:25], s[0:1], 0x28\n"
        "s_load_dwordx8 s[44:51], s[0:1], 0x0\n"
        "s_load_dwordx2 s[20:21], s[0:1], 0x20\n"
        "v_lshlrev_b32 v3, 5, v0\n"
        "v_lshrrev_b32 v8, 3, v0\n"
        "v_mul_u32_u24 v8, 917504, v8\n"
        "v_lshl_add_u32 v8, v0, 4, v8\n"
        "v_mov_b32 v4, 0\n"
        "v_mov_b32 v5, 0x1e400000\n"
        "v_mov_b32 v6, 0\n"
        "v_mov_b32 v7, v5\n"
        "v_lshlrev_b32 v9, 4, v0\n"
        "v_mov_b32 v32, 0\n"
        "v_mov_b32 v33, 0\n"
        "v_mov_b32 v34, 0\n"
        "v_mov_b32 v35, 0\n"
        "s_mov_b32 s27, s31\n"
        "s_mov_b32 s26, 163840\n"
        "v_mul_u32_u24 v2, 2048, v0\n"
        "s_mov_b32 s40, 32768\n"
        "s_waitcnt lgkmcnt(0)\n"
        "s_load_dwordx2 s[34:35], s[24:25], 0x0\n"
        "global_load_dwordx4 v[12:15], v3, s[44:45]\n"
        "global_load_dwordx4 v[16:19], v3, s[44:45] offset:16\n"
        "global_load_dwordx4 v[20:23], v8, s[46:47]\n"
        "global_load_dwordx4 v[24:27], v8, s[48:49]\n"
        "global_load_dwordx4 v[28:31], v8, s[50:51]\n"
        "s_movk_i32 s41, 16384\n"
        "buffer_store_dwordx4 v[4:7], v9, s[24:27], s41 offen offset:0 sc1\n"
        "buffer_store_dwordx4 v[4:7], v9, s[24:27], s41 offen offset:1024 sc1\n"
        "buffer_store_dwordx4 v[4:7], v9, s[24:27], s41 offen offset:2048 sc1\n"
        "buffer_store_dwordx4 v[4:7], v9, s[24:27], s41 offen offset:3072 sc1\n"
        "s_addk_i32 s41, 4096\n"
        "buffer_store_dwordx4 v[4:7], v9, s[24:27], s41 offen offset:0 sc1\n"
        "buffer_store_dwordx4 v[4:7], v9, s[24:27], s41 offen offset:1024 sc1\n"
        "buffer_store_dwordx4 v[4:7], v9, s[24:27], s41 offen offset:2048 sc1\n"
        "buffer_store_dwordx4 v[4:7], v9, s[24:27], s41 offen offset:3072 sc1\n"
        "s_addk_i32 s41, 4096\n"
        "buffer_store_dwordx4 v[4:7], v9, s[24:27], s41 offen offset:0 sc1\n"
        "buffer_store_dwordx4 v[4:7], v9, s[24:27], s41 offen offset:1024 sc1\n"
        "buffer_store_dwordx4 v[4:7], v9, s[24:27], s41 offen offset:2048 sc1\n"
        "buffer_store_dwordx4 v[4:7], v9, s[24:27], s41 offen offset:3072 sc1\n"
        "s_addk_i32 s41, 4096\n"
        "buffer_store_dwordx4 v[4:7], v9, s[24:27], s41 offen offset:0 sc1\n"
        "buffer_store_dwordx4 v[4:7], v9, s[24:27], s41 offen offset:1024 sc1\n"
        "buffer_store_dwordx4 v[4:7], v9, s[24:27], s41 offen offset:2048 sc1\n"
        "buffer_store_dwordx4 v[4:7], v9, s[24:27], s41 offen offset:3072 sc1\n"
        "global_store_dwordx4 v9, v[4:7], s[20:21] sc1\n"
        "global_store_dwordx4 v9, v[4:7], s[20:21] offset:1024 sc1\n"
        "s_waitcnt vmcnt(18) lgkmcnt(0)\n"
        "v_dot2c_f32_bf16 v32, v12, v12\n"
        "v_dot2c_f32_bf16 v32, v13, v13\n"
        "v_dot2c_f32_bf16 v32, v14, v14\n"
        "v_dot2c_f32_bf16 v32, v15, v15\n"
        "v_dot2c_f32_bf16 v32, v16, v16\n"
        "v_dot2c_f32_bf16 v32, v17, v17\n"
        "v_dot2c_f32_bf16 v32, v18, v18\n"
        "v_dot2c_f32_bf16 v32, v19, v19\n"
        "v_dot2c_f32_bf16 v33, v20, v20\n"
        "v_dot2c_f32_bf16 v33, v21, v21\n"
        "v_dot2c_f32_bf16 v33, v22, v22\n"
        "v_dot2c_f32_bf16 v33, v23, v23\n"
        "v_dot2c_f32_bf16 v34, v24, v24\n"
        "v_dot2c_f32_bf16 v34, v25, v25\n"
        "v_dot2c_f32_bf16 v34, v26, v26\n"
        "v_dot2c_f32_bf16 v34, v27, v27\n"
        "v_dot2c_f32_bf16 v35, v28, v28\n"
        "v_dot2c_f32_bf16 v35, v29, v29\n"
        "v_dot2c_f32_bf16 v35, v30, v30\n"
        "v_dot2c_f32_bf16 v35, v31, v31\n"
        "s_and_b32 s56, s34, 0x7f7f7f7f\n"
        "s_and_b32 s57, s35, 0x7f7f7f7f\n"
        "s_add_u32 s56, s56, 0x01010101\n"
        "s_add_u32 s57, s57, 0x01010101\n"
        "s_and_b32 s34, s34, 0x80808080\n"
        "s_and_b32 s35, s35, 0x80808080\n"
        "s_xor_b64 s[34:35], s[34:35], s[56:57]\n"
        "v_permlane32_swap_b32 v32, v33\n"
        "v_permlane32_swap_b32 v34, v35\n"
        "s_nop 1\n"
        "v_add_f32 v32, v32, v33\n"
        "v_add_f32 v34, v34, v35\n"
        "s_nop 1\n"
        "v_permlane16_swap_b32 v32, v34\n"
        "s_nop 1\n"
        "v_add_f32 v32, v32, v34\n"
        "s_nop 1\n"
        "v_add_f32_dpp v32, v32, v32 row_ror:8 row_mask:0xf bank_mask:0xf\n"
        "s_nop 1\n"
        "v_add_f32_dpp v32, v32, v32 row_ror:4 row_mask:0xf bank_mask:0xf\n"
        "s_nop 1\n"
        "v_add_f32_dpp v32, v32, v32 row_ror:2 row_mask:0xf bank_mask:0xf\n"
        "s_nop 1\n"
        "v_add_f32_dpp v32, v32, v32 row_ror:1 row_mask:0xf bank_mask:0xf\n"
        "v_bfe_u32 v32, v32, 23, 8\n"
        "s_nop 0\n"
        "v_readlane_b32 s56, v32, 0\n"
        "v_readlane_b32 s57, v32, 16\n"
        "v_readlane_b32 s58, v32, 32\n"
        "v_readlane_b32 s59, v32, 48\n"
        "s_add_i32 s57, s57, s58\n"
        "s_add_i32 s57, s57, s59\n"
        "s_lshr_b32 s57, s57, 1\n"
        "s_add_i32 s56, s56, s57\n"
        "s_sub_i32 s56, 1400, s56\n"
        "s_lshl_b32 s56, s56, 20\n"
        "s_pack_lh_b32_b16 s57, s34, s56\n"
        "s_pack_hh_b32_b16 s58, s34, s56\n"
        "s_pack_lh_b32_b16 s59, s35, s56\n"
        "s_pack_hh_b32_b16 s60, s35, s56\n"
        "v_mov_b32 v12, s57\n"
        "v_mov_b32 v13, s58\n"
        "v_mov_b32 v14, s59\n"
        "v_mov_b32 v15, s60\n"
        "v_lshlrev_b32 v3, 7, v0\n"
        "s_movk_i32 s41, 4096\n"
        "s_mov_b32 s50, 0\n"
        "s_waitcnt vmcnt(0)\n"
        "buffer_store_dwordx4 v[12:15], v3, s[24:27], s41 offen sc1\n"
        "s_cbranch_execz .Lglu_EXIT\n"
        "s_mov_b32 exec_hi, 0xffffff7f\n"
        "s_sleep 30\n"
        "s_waitcnt lgkmcnt(0)\n"
        ".Lglu_CPOLL:\n"
        "s_sleep 8\n"
        "buffer_load_dwordx2 v[4:5], v2, s[24:27], s40 offen offset:256 sc1\n"
        "buffer_load_dwordx2 v[6:7], v2, s[24:27], s40 offen offset:768 sc1\n"
        "buffer_load_dwordx2 v[8:9], v2, s[24:27], s40 offen offset:1280 sc1\n"
        "buffer_load_dwordx2 v[10:11], v2, s[24:27], s40 offen offset:1792 sc1\n"
        "s_waitcnt vmcnt(0)\n"
        "v_cmp_ne_u64 s[56:57], s[34:35], v[4:5]\n"
        "v_cmp_ne_u64 s[58:59], s[34:35], v[6:7]\n"
        "v_cmp_ne_u64 s[60:61], s[34:35], v[8:9]\n"
        "v_cmp_ne_u64 vcc, s[34:35], v[10:11]\n"
        "s_or_b64 s[60:61], s[60:61], vcc\n"
        "s_or_b64 s[56:57], s[56:57], s[58:59]\n"
        "s_or_b64 s[56:57], s[56:57], s[60:61]\n"
        "s_cbranch_scc0 .Lglu_CDONE\n"
        "s_add_u32 s50, s50, 1\n"
        "s_cmp_lt_u32 s50, 2000000\n"
        "s_cbranch_scc1 .Lglu_CPOLL\n"
        ".Lglu_CDONE:\n"
        "s_mov_b64 exec, 1\n"
        "v_mov_b32 v4, s34\n"
        "v_mov_b32 v5, s35\n"
        "buffer_store_dwordx2 v[4:5], off, s[24:27], 0 sc1\n"
        "s_endpgm\n"
        ".Lglu_FB:\n"
        "s_mov_b64 exec, -1\n"
        "v_mov_b32 v20, 0\n"
        "s_mov_b32 s50, 0\n"
        "s_add_i32 s60, s57, 236544\n"
        ".Lglu_FB1:\n"
        "s_mov_b32 s56, s57\n"
        "buffer_load_dword v24, v107, s[24:27], s56 offen sc1\n"
        "s_addk_i32 s56, 4224\n"
        "buffer_load_dword v25, v107, s[24:27], s56 offen sc1\n"
        "s_addk_i32 s56, 4224\n"
        "buffer_load_dword v26, v107, s[24:27], s56 offen sc1\n"
        "s_addk_i32 s56, 4224\n"
        "buffer_load_dword v27, v107, s[24:27], s56 offen sc1\n"
        "s_addk_i32 s56, 4224\n"
        "buffer_load_dword v28, v107, s[24:27], s56 offen sc1\n"
        "s_addk_i32 s56, 4224\n"
        "buffer_load_dword v29, v107, s[24:27], s56 offen sc1\n"
        "s_addk_i32 s56, 4224\n"
        "buffer_load_dword v30, v107, s[24:27], s56 offen sc1\n"
        "s_addk_i32 s56, 4224\n"
        "buffer_load_dword v31, v107, s[24:27], s56 offen sc1\n"
        "s_addk_i32 s56, 4224\n"
        "s_waitcnt vmcnt(0)\n"
        "v_xor_b32 v24, v24, v105\n"
        "v_xor_b32 v25, v25, v105\n"
        "v_xor_b32 v26, v26, v105\n"
        "v_xor_b32 v27, v27, v105\n"
        "v_xor_b32 v28, v28, v105\n"
        "v_xor_b32 v29, v29, v105\n"
        "v_xor_b32 v30, v30, v105\n"
        "v_xor_b32 v31, v31, v105\n"
        "v_or3_b32 v21, v24, v25, v26\n"
        "v_or3_b32 v21, v21, v27, v28\n"
        "v_or3_b32 v21, v21, v29, v30\n"
        "v_or_b32 v21, v21, v31\n"
        "v_cmp_ne_u32_sdwa vcc, v21, 0 src0_sel:BYTE_0 src1_sel:DWORD\n"
        "s_cbranch_vccz .Lglu_FB2\n"
        "s_add_u32 s50, s50, 1\n"
        "s_cmp_lt_u32 s50, 2000000\n"
        "s_cbranch_scc1 .Lglu_FB1\n"
        ".Lglu_FB2:\n"
        "v_add_f32 v20, v20, v24\n"
        "v_add_f32 v20, v20, v25\n"
        "v_add_f32 v20, v20, v26\n"
        "v_add_f32 v20, v20, v27\n"
        "v_add_f32 v20, v20, v28\n"
        "v_add_f32 v20, v20, v29\n"
        "v_add_f32 v20, v20, v30\n"
        "v_add_f32 v20, v20, v31\n"
        "s_mov_b32 s57, s56\n"
        "s_cmp_lt_u32 s57, s60\n"
        "s_cbranch_scc1 .Lglu_FB1\n"
        "s_mov_b64 exec, s[58:59]\n"
        "v_cvt_pk_bf16_f32 v20, v20, v20\n"
        "buffer_store_short v20, v109, s[20:23], s54 offen\n"
        "s_endpgm\n"
        ".Lglu_CODE_END:\n"
        : "+{v0}"(tid)
        : "{s[0:1]}"(__builtin_amdgcn_kernarg_segment_ptr()), "{s2}"(blockIdx.x),
          "{s3}"((unsigned)(size_t)(__attribute__((address_space(3))) unsigned char*)lds)
        : "v1", "v2", "v3", "v4", "v5", "v6", "v7", "v8", "v9", "v10", "v11", "v12", "v13", "v14", "v15", "v16", "v17", "v18", "v19", "v20", "v21", "v22", "v23", "v24", "v25", "v26", "v27", "v28", "v29", "v30", "v31", "v32", "v33", "v34", "v35", "v36", "v37", "v38", "v39", "v40", "v41", "v42", "v43", "v44", "v45", "v46", "v47", "v48", "v49", "v50", "v51", "v52", "v53", "v54", "v55", "v56", "v57", "v58", "v59", "v60", "v61", "v62", "v63", "v64", "v65", "v66", "v67", "v68", "v69", "v70", "v71", "v72", "v73", "v74", "v75", "v76", "v77", "v78", "v79", "v80", "v81", "v82", "v83", "v84", "v85", "v86", "v87", "v88", "v89", "v90", "v91", "v92", "v93", "v94", "v95", "v96", "v97", "v98", "v99", "v100", "v101", "v102", "v103", "v104", "v105", "v106", "v107", "v108", "v109", "v110", "v111", "v112", "v113", "v114", "v115", "v116", "v117", "v118", "v119", "v120", "v121", "v122", "v123", "v124", "v125", "v126", "v127", "s4", "s5", "s6", "s7", "s8", "s9", "s10", "s11", "s12", "s13", "s14", "s15", "s16", "s17", "s18", "s19", "s20", "s21", "s22", "s23", "s24", "s25", "s26", "s27", "s28", "s29", "s30", "s31", "s34", "s35", "s36", "s37", "s38", "s39", "s40", "s41", "s42", "s43", "s44", "s45", "s46", "s47", "s48", "s49", "s50", "s51", "s52", "s53", "s54", "s55", "s56", "s57", "s58", "s59", "s60", "s61", "s62", "s63", "vcc", "scc", "memory");
}

extern "C" void kernel_launch(void* const* d_in, const int* in_sizes, int n_in, void* d_out, int out_size, void* d_ws, size_t ws_size,
                              hipStream_t stream) {
    glu_kernel<<<256, 320, 0, stream>>>(d_in[0], d_in[1], d_in[2], d_in[3], d_out, d_ws);
}
```
